```python
import torch

def _softmax(x, axis=-1):
    return torch.softmax(x, dim=axis)
def _gelu(x):
    return torch.nn.functional.gelu(x, approximate="tanh")
def _silu(x):
    return torch.nn.functional.silu(x)
def _sigmoid(x):
    return torch.sigmoid(x)
def _layernorm(x, axis=-1, eps=1e-5):
    m = x.mean(dim=axis, keepdim=True)
    v = x.var(dim=axis, keepdim=True, unbiased=False)
    return (x - m) / torch.sqrt(v + eps)
def _scatter_add(tgt, idx, src, axis=0):
    return tgt.clone().index_add_(axis, idx, src)
def _segment_sum(src, seg, n):
    out = torch.zeros((n,) + src.shape[1:], dtype=src.dtype)
    return out.index_add_(0, seg, src)
def _pad(x, pw):
    flat = tuple(v for pair in reversed(pw) for v in pair)
    return torch.nn.functional.pad(x, flat)

kernel_name = 'standalone__batchnorm__sampling'

def setup_inputs():

    _w = (lambda a: (lambda t: t.to(torch.bfloat16) if t.is_floating_point() else t)(torch.as_tensor(a)))
    B, S, C = 1024, 32, 512
    K = 3
    return {
        "x": _w((lambda *s: torch.randn(*s, dtype=torch.bfloat16) * 0.05)(B, S, C)),
        "w": _w((lambda *s: torch.randn(*s, dtype=torch.bfloat16) * 0.05)(K, C, C) * (1.0 / (K * C) ** 0.5)),
        "mean": _w((lambda *s: torch.randn(*s, dtype=torch.bfloat16) * 0.05)(C) * 0.1),
        "var": _w(torch.abs((lambda *s: torch.randn(*s, dtype=torch.bfloat16) * 0.05)(C)) + 0.5),
        "g": _w((lambda *s: torch.randn(*s, dtype=torch.bfloat16) * 0.05)(C) + 1.0),
        "b": _w((lambda *s: torch.randn(*s, dtype=torch.bfloat16) * 0.05)(C) * 0.1),
    }

def reference(x, w, mean, var, g, b):
    K = 3
    P = K // 2
    S = x.shape[1]
    xp = _pad(x, ((0, 0), (P, P), (0, 0)))
    y = sum(torch.einsum('bsc,cd->bsd', xp[:, k:k + S, :], w[k]) for k in range(K))
    inv_std = 1.0 / torch.sqrt(var + 1e-5)
    y = (y - mean) * (g * inv_std) + b
    return _silu(y)
```

```cpp
#include <hip/hip_runtime.h>
__global__ __launch_bounds__(256, 1) void conv_bn_silu_kernel(const unsigned short* x, const unsigned short* w, const unsigned short* mean,
    const unsigned short* var, const unsigned short* g, const unsigned short* b, unsigned short* out) {
  __shared__ __attribute__((aligned(128))) char smem[151744];
  const unsigned lb = (unsigned)(unsigned long long)(__attribute__((address_space(3))) char*)smem;
  asm volatile(
    "s_load_dwordx2 s[56:57], %[ka], 0x0\n"
    "s_load_dwordx2 s[60:61], %[ka], 0x8\n"
    "s_load_dwordx8 s[76:83], %[ka], 0x10\n"
    "s_load_dwordx2 s[64:65], %[ka], 0x30\n"
    "s_getpc_b64 s[68:69]\n"
    "CBS_PFB:\n"
    "s_mov_b32 s70, CBS_ENDL-CBS_PFB\n"
    "s_mov_b32 s71, 0x20000\n"
    "v_lshlrev_b32 v29, 7, %[tid]\n"
    "buffer_load_dword v30, v29, s[68:71], 0 offen\n"
    "v_readfirstlane_b32 s54, %[tid]\n"
    "s_and_b32 s55, %[bid], 7\n"
    "s_lshl_b32 s55, s55, 22\n"
    "s_lshr_b32 s43, %[bid], 5\n"
    "s_lshl_b32 s43, s43, 19\n"
    "s_add_u32 s55, s55, s43\n"
    "s_lshr_b32 s54, s54, 6\n"
    "s_mov_b32 s58, 33554432\n"
    "s_mov_b32 s59, 0x20000\n"
    "s_lshr_b32 s44, s54, 1\n"
    "s_mul_i32 s43, s44, 7\n"
    "v_bfe_u32 v23, %[tid], 3, 3\n"
    "v_xor_b32 v23, s43, v23\n"
    "v_and_b32 v24, 7, %[tid]\n"
    "v_lshlrev_b32 v24, 4, v24\n"
    "v_lshl_add_u32 v14, v23, 10, v24\n"
    "v_add_u32 v15, 31712, v14\n"
    "v_add_u32 v16, 63424, v14\n"
    "v_add_u32 v17, 95136, v14\n"
    "s_mul_i32 s49, s54, 8448\n"
    "s_and_b32 s53, s54, 1\n"
    "s_lshl_b32 s47, s53, 18\n"
    "s_add_u32 s47, s47, s55\n"
    "s_mul_i32 s43, s44, 24576\n"
    "s_add_u32 s46, s47, s43\n"
    "s_mov_b32 s40, %[lb]\n"
    "s_bfe_u32 s52, %[bid], 0x20003\n"
    "s_lshl_b32 s52, s52, 8\n"
    "v_and_b32 v25, 127, %[tid]\n"
    "v_lshl_add_u32 v22, v25, 1, s52\n"
    "s_mov_b32 s62, 1572864\n"
    "s_mov_b32 s63, 0x20000\n"
    "v_and_b32 v26, 15, %[tid]\n"
    "v_bfe_u32 v27, %[tid], 4, 2\n"
    "v_lshlrev_b32 v23, 2, v27\n"
    "v_xor_b32 v23, v23, v26\n"
    "v_lshlrev_b32 v23, 4, v23\n"
    "v_lshl_add_u32 v12, v27, 10, v23\n"
    "v_add_u32 v13, 3072, v12\n"
    "s_mul_i32 s50, s54, 2056\n"
    "s_add_u32 s50, s50, 101376\n"
    "s_add_u32 s50, s50, %[lb]\n"
    "s_lshl_b32 s48, s54, 13\n"
    "s_add_u32 s48, s48, s52\n"
    "s_lshl_b32 s43, s44, 13\n"
    "s_add_u32 s47, s47, s43\n"
    "s_add_u32 s47, s47, 8192\n"
    "s_add_u32 s41, s40, 33792\n"
    "s_add_u32 s42, s40, 67584\n"
    "v_mov_b32 v18, 0\n"
    "v_mov_b32 v19, 0\n"
    "v_mov_b32 v20, 0\n"
    "v_mov_b32 v21, 0\n"
    "v_mul_u32_u24 v23, 1056, v26\n"
    "v_lshl_add_u32 v23, v27, 4, v23\n"
    "s_mul_i32 s43, s44, 16896\n"
    "v_add_u32 v4, s43, v23\n"
    "s_sub_u32 s43, 16896, s43\n"
    "v_add_u32 v5, s43, v23\n"
    "v_add_u32 v2, s40, v4\n"
    "v_bfe_u32 v23, %[tid], 2, 2\n"
    "v_and_b32 v24, 3, %[tid]\n"
    "v_mul_u32_u24 v28, 2056, v27\n"
    "v_lshl_add_u32 v28, v23, 8, v28\n"
    "v_lshl_add_u32 v28, v24, 4, v28\n"
    "s_lshl_b32 s43, s53, 1\n"
    "v_xor_b32 v23, s43, v23\n"
    "v_xor_b32 v24, 1, v23\n"
    "v_lshl_add_u32 v23, v23, 6, v28\n"
    "v_lshl_add_u32 v24, v24, 6, v28\n"
    "s_add_u32 s51, %[lb], 117824\n"
    "v_add_u32 v10, s51, v23\n"
    "v_add_u32 v11, s51, v24\n"
    "s_mul_i32 s43, s44, 32896\n"
    "s_sub_u32 s43, s43, 16448\n"
    "v_add_u32 v6, s43, v10\n"
    "v_add_u32 v7, s43, v11\n"
    "v_subrev_u32 v8, s43, v10\n"
    "v_subrev_u32 v9, s43, v11\n"
    "s_mov_b32 s66, 33554432\n"
    "s_mov_b32 s67, 0x20000\n"
    "s_lshl_b32 s43, s53, 7\n"
    "v_lshl_add_u32 v23, v27, 4, s43\n"
    "v_lshl_add_u32 v1, v26, 15, v23\n"
    "s_mul_i32 s51, s44, 31744\n"
    "s_add_u32 s51, s51, s55\n"
    "s_add_u32 s51, s51, s52\n"
    "s_lshl_b32 s52, s44, 11\n"
    "s_sub_u32 s52, 1024, s52\n"
    "v_lshl_add_u32 v23, v23, 1, %[lb]\n"
    "v_add_u32 v23, 150720, v23\n"
    "v_lshl_add_u32 v25, v25, 2, %[lb]\n"
    "v_add_u32 v25, 150720, v25\n"
    "s_waitcnt lgkmcnt(0)\n"
    "global_load_ushort v32, v22, s[76:77]\n"
    "global_load_ushort v33, v22, s[78:79]\n"
    "global_load_ushort v34, v22, s[80:81]\n"
    "global_load_ushort v35, v22, s[82:83]\n"
    "s_add_u32 m0, s40, s49\n"
    "s_add_u32 s43, s46, 0\n"
    "buffer_load_dwordx4 v14, s[56:59], s43 offen offset:0  lds\n"
    "buffer_load_dwordx4 v15, s[56:59], s43 offen offset:1056  lds\n"
    "buffer_load_dwordx4 v16, s[56:59], s43 offen offset:2112  lds\n"
    "buffer_load_dwordx4 v17, s[56:59], s43 offen offset:3168  lds\n"
    "s_add_u32 m0, m0, 4224\n"
    "s_add_u32 s43, s46, 131072\n"
    "buffer_load_dwordx4 v14, s[56:59], s43 offen offset:0  lds\n"
    "v_mfma_f32_32x32x16_bf16 a[184:199], v[18:21], v[18:21], 0\n"
    "buffer_load_dwordx4 v15, s[56:59], s43 offen offset:1056  lds\n"
    "v_mfma_f32_32x32x16_bf16 a[168:183], v[18:21], v[18:21], 0\n"
    "buffer_load_dwordx4 v16, s[56:59], s43 offen offset:2112  lds\n"
    "v_mfma_f32_32x32x16_bf16 a[152:167], v[18:21], v[18:21], 0\n"
    "buffer_load_dwordx4 v17, s[56:59], s43 offen offset:3168  lds\n"
    "v_mfma_f32_32x32x16_bf16 a[136:151], v[18:21], v[18:21], 0\n"
    "s_add_u32 m0, s50, 16448\n"
    "s_add_u32 s43, s48, 524288\n"
    "buffer_load_dwordx4 v12, s[60:63], s43 offen offset:0  lds\n"
    "v_mfma_f32_32x32x16_bf16 v[240:255], v[18:21], v[18:21], 0\n"
    "buffer_load_dwordx4 v13, s[60:63], s43 offen offset:1024  lds\n"
    "v_mfma_f32_32x32x16_bf16 v[224:239], v[18:21], v[18:21], 0\n"
    "s_add_u32 m0, s50, 0\n"
    "s_add_u32 s43, s48, 0\n"
    "buffer_load_dwordx4 v12, s[60:63], s43 offen offset:0  lds\n"
    "v_mfma_f32_32x32x16_bf16 v[208:223], v[18:21], v[18:21], 0\n"
    "buffer_load_dwordx4 v13, s[60:63], s43 offen offset:1024  lds\n"
    "v_mfma_f32_32x32x16_bf16 v[192:207], v[18:21], v[18:21], 0\n"
    "s_add_u32 m0, s50, 32896\n"
    "s_add_u32 s43, s48, 1048576\n"
    "buffer_load_dwordx4 v12, s[60:63], s43 offen offset:0  lds\n"
    "v_mfma_f32_32x32x16_bf16 v[176:191], v[18:21], v[18:21], 0\n"
    "buffer_load_dwordx4 v13, s[60:63], s43 offen offset:1024  lds\n"
    "v_mfma_f32_32x32x16_bf16 v[160:175], v[18:21], v[18:21], 0\n"
    "s_add_u32 m0, s50, 24672\n"
    "s_add_u32 s43, s48, 557056\n"
    "buffer_load_dwordx4 v12, s[60:63], s43 offen offset:0  lds\n"
    "v_mfma_f32_32x32x16_bf16 v[144:159], v[18:21], v[18:21], 0\n"
    "buffer_load_dwordx4 v13, s[60:63], s43 offen offset:1024  lds\n"
    "v_mfma_f32_32x32x16_bf16 v[128:143], v[18:21], v[18:21], 0\n"
    "s_add_u32 m0, s50, 8224\n"
    "s_add_u32 s43, s48, 32768\n"
    "buffer_load_dwordx4 v12, s[60:63], s43 offen offset:0  lds\n"
    "v_mfma_f32_32x32x16_bf16 v[112:127], v[18:21], v[18:21], 0\n"
    "buffer_load_dwordx4 v13, s[60:63], s43 offen offset:1024  lds\n"
    "v_mfma_f32_32x32x16_bf16 v[96:111], v[18:21], v[18:21], 0\n"
    "s_add_u32 m0, s50, 41120\n"
    "s_add_u32 s43, s48, 1081344\n"
    "buffer_load_dwordx4 v12, s[60:63], s43 offen offset:0  lds\n"
    "v_mfma_f32_32x32x16_bf16 v[80:95], v[18:21], v[18:21], 0\n"
    "buffer_load_dwordx4 v13, s[60:63], s43 offen offset:1024  lds\n"
    "v_mfma_f32_32x32x16_bf16 v[64:79], v[18:21], v[18:21], 0\n"
    "s_waitcnt vmcnt(20)\n"
    "v_lshlrev_b32 v32, 16, v32\n"
    "v_lshlrev_b32 v33, 16, v33\n"
    "v_lshlrev_b32 v34, 16, v34\n"
    "v_lshlrev_b32 v35, 16, v35\n"
    "v_add_f32 v33, 0x3727c5ac, v33\n"
    "v_cvt_pk_bf16_f32 v33, v33, v33\n"
    "v_lshlrev_b32 v33, 16, v33\n"
    "v_sqrt_f32 v33, v33\n"
    "s_nop 0\n"
    "v_cvt_pk_bf16_f32 v33, v33, v33\n"
    "v_lshlrev_b32 v33, 16, v33\n"
    "v_rcp_f32 v33, v33\n"
    "s_nop 0\n"
    "v_cvt_pk_bf16_f32 v33, v33, v33\n"
    "v_lshlrev_b32 v33, 16, v33\n"
    "v_mul_f32 v33, v34, v33\n"
    "v_cvt_pk_bf16_f32 v33, v33, v33\n"
    "v_lshlrev_b32 v33, 16, v33\n"
    "v_fma_f32 v35, -v32, v33, v35\n"
    "v_mul_f32 v33, 0xbfb8aa3b, v33\n"
    "v_mul_f32 v35, 0xbfb8aa3b, v35\n"
    "ds_write_b32 v25, v33\n"
    "ds_write_b32 v25, v35 offset:512\n"
    "s_mov_b32 s54, 0xbfb8aa3b\n"
    "s_mov_b32 s55, 0xbfb8aa3b\n"
    "s_mov_b32 s45, 0\n"
    "s_add_u32 m0, s41, s49\n"
    "s_add_u32 s43, s47, 0\n"
    "buffer_load_dwordx4 v14, s[56:59], s43 offen offset:0  lds\n"
    "buffer_load_dwordx4 v15, s[56:59], s43 offen offset:1056  lds\n"
    "buffer_load_dwordx4 v16, s[56:59], s43 offen offset:2112  lds\n"
    "buffer_load_dwordx4 v17, s[56:59], s43 offen offset:3168  lds\n"
    "s_add_u32 m0, m0, 4224\n"
    "s_add_u32 s43, s47, 131072\n"
    "buffer_load_dwordx4 v14, s[56:59], s43 offen offset:0  lds\n"
    "buffer_load_dwordx4 v15, s[56:59], s43 offen offset:1056  lds\n"
    "buffer_load_dwordx4 v16, s[56:59], s43 offen offset:2112  lds\n"
    "buffer_load_dwordx4 v17, s[56:59], s43 offen offset:3168  lds\n"
    "s_add_u32 s46, s46, 128\n"
    "s_add_u32 s47, s47, 128\n"
    "s_add_u32 s48, s48, 65536\n"
    "s_waitcnt vmcnt(8) lgkmcnt(0)\n"
    "s_barrier\n"
    "ds_read_b128 v[32:35], v23 offset:0\n"
    "ds_read_b128 v[36:39], v23 offset:16\n"
    "ds_read_b128 v[40:43], v23 offset:128\n"
    "ds_read_b128 v[44:47], v23 offset:144\n"
    "ds_read_b128 v[48:51], v23 offset:512\n"
    "ds_read_b128 v[52:55], v23 offset:528\n"
    "ds_read_b128 v[56:59], v23 offset:640\n"
    "ds_read_b128 v[60:63], v23 offset:656\n"
    "ds_read_b128 a[96:99], v2 offset:0\n"
    "ds_read_b128 a[100:103], v2 offset:64\n"
    "ds_read_b64_tr_b16 a[32:33], v10 offset:0\n"
    "ds_read_b64_tr_b16 a[34:35], v10 offset:1024\n"
    "ds_read_b64_tr_b16 a[48:49], v10 offset:8224\n"
    "ds_read_b64_tr_b16 a[50:51], v10 offset:9248\n"
    "ds_read_b64_tr_b16 a[64:65], v8 offset:0\n"
    "ds_read_b64_tr_b16 a[66:67], v8 offset:1024\n"
    "ds_read_b128 a[104:107], v2 offset:128\n"
    "ds_read_b64_tr_b16 a[80:81], v8 offset:8224\n"
    "ds_read_b64_tr_b16 a[82:83], v8 offset:9248\n"
    "ds_read_b128 a[108:111], v2 offset:192\n"
    "ds_read_b64_tr_b16 a[36:37], v10 offset:8\n"
    "ds_read_b64_tr_b16 a[38:39], v10 offset:1032\n"
    "ds_read_b64_tr_b16 a[52:53], v10 offset:8232\n"
    "ds_read_b64_tr_b16 a[54:55], v10 offset:9256\n"
    "ds_read_b64_tr_b16 a[68:69], v8 offset:8\n"
    "ds_read_b64_tr_b16 a[70:71], v8 offset:1032\n"
    "ds_read_b64_tr_b16 a[84:85], v8 offset:8232\n"
    "ds_read_b64_tr_b16 a[86:87], v8 offset:9256\n"
    "ds_read_b64_tr_b16 a[40:41], v11 offset:0\n"
    "ds_read_b64_tr_b16 a[42:43], v11 offset:1024\n"
    "ds_read_b64_tr_b16 a[56:57], v11 offset:8224\n"
    "ds_read_b64_tr_b16 a[58:59], v11 offset:9248\n"
    "ds_read_b64_tr_b16 a[72:73], v9 offset:0\n"
    "ds_read_b64_tr_b16 a[74:75], v9 offset:1024\n"
    "ds_read_b64_tr_b16 a[88:89], v9 offset:8224\n"
    "ds_read_b64_tr_b16 a[90:91], v9 offset:9248\n"
    "ds_read_b64_tr_b16 a[44:45], v11 offset:8\n"
    "ds_read_b64_tr_b16 a[46:47], v11 offset:1032\n"
    "ds_read_b64_tr_b16 a[60:61], v11 offset:8232\n"
    "ds_read_b64_tr_b16 a[62:63], v11 offset:9256\n"
    "CBS_LOOP:\n"
    "s_waitcnt lgkmcnt(10)\n"
    "v_mfma_f32_16x16x32_bf16 v[64:67], a[32:35], a[96:99], v[64:67]\n"
    "ds_read_b64_tr_b16 a[76:77], v9 offset:8\n"
    "ds_read_b64_tr_b16 a[78:79], v9 offset:1032\n"
    "v_mfma_f32_16x16x32_bf16 v[64:67], a[48:51], a[100:103], v[64:67]\n"
    "ds_read_b64_tr_b16 a[92:93], v9 offset:8232\n"
    "v_mfma_f32_16x16x32_bf16 v[64:67], a[64:67], a[104:107], v[64:67]\n"
    "ds_read_b64_tr_b16 a[94:95], v9 offset:9256\n"
    "v_mfma_f32_16x16x32_bf16 v[64:67], a[80:83], a[108:111], v[64:67]\n"
    "ds_read_b128 a[112:115], v2 offset:256\n"
    "ds_read_b128 a[116:119], v2 offset:320\n"
    "v_mfma_f32_16x16x32_bf16 v[68:71], a[36:39], a[96:99], v[68:71]\n"
    "ds_read_b64_tr_b16 a[0:1], v6 offset:0\n"
    "v_mfma_f32_16x16x32_bf16 v[68:71], a[52:55], a[100:103], v[68:71]\n"
    "ds_read_b64_tr_b16 a[2:3], v6 offset:1024\n"
    "v_mfma_f32_16x16x32_bf16 v[68:71], a[68:71], a[104:107], v[68:71]\n"
    "ds_read_b64_tr_b16 a[16:17], v6 offset:8224\n"
    "ds_read_b64_tr_b16 a[18:19], v6 offset:9248\n"
    "v_mfma_f32_16x16x32_bf16 v[68:71], a[84:87], a[108:111], v[68:71]\n"
    "ds_read_b64_tr_b16 a[4:5], v6 offset:8\n"
    "v_mfma_f32_16x16x32_bf16 v[72:75], a[40:43], a[96:99], v[72:75]\n"
    "ds_read_b64_tr_b16 a[6:7], v6 offset:1032\n"
    "s_waitcnt lgkmcnt(10)\n"
    "v_mfma_f32_16x16x32_bf16 v[72:75], a[56:59], a[100:103], v[72:75]\n"
    "ds_read_b64_tr_b16 a[20:21], v6 offset:8232\n"
    "s_add_u32 m0, s42, s49\n"
    "v_mfma_f32_16x16x32_bf16 v[72:75], a[72:75], a[104:107], v[72:75]\n"
    "ds_read_b64_tr_b16 a[22:23], v6 offset:9256\n"
    "s_add_u32 s43, s46, 0\n"
    "v_mfma_f32_16x16x32_bf16 v[72:75], a[88:91], a[108:111], v[72:75]\n"
    "ds_read_b64_tr_b16 a[8:9], v7 offset:0\n"
    "buffer_load_dwordx4 v14, s[56:59], s43 offen offset:0  lds\n"
    "v_mfma_f32_16x16x32_bf16 v[76:79], a[44:47], a[96:99], v[76:79]\n"
    "ds_read_b64_tr_b16 a[10:11], v7 offset:1024\n"
    "v_mfma_f32_16x16x32_bf16 v[76:79], a[60:63], a[100:103], v[76:79]\n"
    "ds_read_b64_tr_b16 a[24:25], v7 offset:8224\n"
    "v_mfma_f32_16x16x32_bf16 v[76:79], a[76:79], a[104:107], v[76:79]\n"
    "ds_read_b64_tr_b16 a[26:27], v7 offset:9248\n"
    "buffer_load_dwordx4 v15, s[56:59], s43 offen offset:1056  lds\n"
    "s_waitcnt lgkmcnt(8)\n"
    "v_mfma_f32_16x16x32_bf16 v[76:79], a[92:95], a[108:111], v[76:79]\n"
    "ds_read_b64_tr_b16 a[12:13], v7 offset:8\n"
    "v_mfma_f32_16x16x32_bf16 v[80:83], a[32:35], a[104:107], v[80:83]\n"
    "ds_read_b64_tr_b16 a[14:15], v7 offset:1032\n"
    "v_mfma_f32_16x16x32_bf16 v[80:83], a[48:51], a[108:111], v[80:83]\n"
    "ds_read_b64_tr_b16 a[28:29], v7 offset:8232\n"
    "buffer_load_dwordx4 v16, s[56:59], s43 offen offset:2112  lds\n"
    "v_mfma_f32_16x16x32_bf16 v[80:83], a[64:67], a[112:115], v[80:83]\n"
    "ds_read_b64_tr_b16 a[30:31], v7 offset:9256\n"
    "v_mfma_f32_16x16x32_bf16 v[80:83], a[80:83], a[116:119], v[80:83]\n"
    "ds_read_b128 a[120:123], v2 offset:384\n"
    "v_mfma_f32_16x16x32_bf16 v[80:83], a[0:3], a[96:99], v[80:83]\n"
    "ds_read_b128 a[124:127], v2 offset:448\n"
    "buffer_load_dwordx4 v17, s[56:59], s43 offen offset:3168  lds\n"
    "v_mfma_f32_16x16x32_bf16 v[80:83], a[16:19], a[100:103], v[80:83]\n"
    "v_mfma_f32_16x16x32_bf16 v[84:87], a[36:39], a[104:107], v[84:87]\n"
    "v_mfma_f32_16x16x32_bf16 v[84:87], a[52:55], a[108:111], v[84:87]\n"
    "s_add_u32 m0, m0, 4224\n"
    "v_mfma_f32_16x16x32_bf16 v[84:87], a[68:71], a[112:115], v[84:87]\n"
    "s_add_u32 s43, s46, 131072\n"
    "v_mfma_f32_16x16x32_bf16 v[84:87], a[84:87], a[116:119], v[84:87]\n"
    "buffer_load_dwordx4 v14, s[56:59], s43 offen offset:0  lds\n"
    "s_waitcnt lgkmcnt(3)\n"
    "v_mfma_f32_16x16x32_bf16 v[84:87], a[4:7], a[96:99], v[84:87]\n"
    "v_mfma_f32_16x16x32_bf16 v[84:87], a[20:23], a[100:103], v[84:87]\n"
    "v_mfma_f32_16x16x32_bf16 v[88:91], a[40:43], a[104:107], v[88:91]\n"
    "buffer_load_dwordx4 v15, s[56:59], s43 offen offset:1056  lds\n"
    "v_mfma_f32_16x16x32_bf16 v[88:91], a[56:59], a[108:111], v[88:91]\n"
    "v_mfma_f32_16x16x32_bf16 v[88:91], a[72:75], a[112:115], v[88:91]\n"
    "v_mfma_f32_16x16x32_bf16 v[88:91], a[88:91], a[116:119], v[88:91]\n"
    "buffer_load_dwordx4 v16, s[56:59], s43 offen offset:2112  lds\n"
    "v_mfma_f32_16x16x32_bf16 v[88:91], a[8:11], a[96:99], v[88:91]\n"
    "v_mfma_f32_16x16x32_bf16 v[88:91], a[24:27], a[100:103], v[88:91]\n"
    "v_mfma_f32_16x16x32_bf16 v[92:95], a[44:47], a[104:107], v[92:95]\n"
    "s_waitcnt lgkmcnt(0)\n"
    "s_barrier\n"
    "v_mfma_f32_16x16x32_bf16 v[92:95], a[60:63], a[108:111], v[92:95]\n"
    "buffer_load_dwordx4 v17, s[56:59], s43 offen offset:3168  lds\n"
    "v_mfma_f32_16x16x32_bf16 v[92:95], a[76:79], a[112:115], v[92:95]\n"
    "v_mfma_f32_16x16x32_bf16 v[92:95], a[92:95], a[116:119], v[92:95]\n"
    "v_mfma_f32_16x16x32_bf16 v[92:95], a[12:15], a[96:99], v[92:95]\n"
    "s_add_u32 m0, s50, 16448\n"
    "v_mfma_f32_16x16x32_bf16 v[92:95], a[28:31], a[100:103], v[92:95]\n"
    "s_add_u32 s43, s48, 524288\n"
    "v_mfma_f32_16x16x32_bf16 v[96:99], a[32:35], a[112:115], v[96:99]\n"
    "buffer_load_dwordx4 v12, s[60:63], s43 offen offset:0  lds\n"
    "v_mfma_f32_16x16x32_bf16 v[96:99], a[48:51], a[116:119], v[96:99]\n"
    "v_mfma_f32_16x16x32_bf16 v[96:99], a[64:67], a[120:123], v[96:99]\n"
    "v_mfma_f32_16x16x32_bf16 v[96:99], a[80:83], a[124:127], v[96:99]\n"
    "ds_read_b128 a[96:99], v2 offset:512\n"
    "v_mfma_f32_16x16x32_bf16 v[96:99], a[0:3], a[104:107], v[96:99]\n"
    "v_mfma_f32_16x16x32_bf16 v[96:99], a[16:19], a[108:111], v[96:99]\n"
    "ds_read_b128 a[100:103], v2 offset:576\n"
    "v_mfma_f32_16x16x32_bf16 v[100:103], a[36:39], a[112:115], v[100:103]\n"
    "buffer_load_dwordx4 v13, s[60:63], s43 offen offset:1024  lds\n"
    "v_mfma_f32_16x16x32_bf16 v[100:103], a[52:55], a[116:119], v[100:103]\n"
    "v_mfma_f32_16x16x32_bf16 v[100:103], a[68:71], a[120:123], v[100:103]\n"
    "v_mfma_f32_16x16x32_bf16 v[100:103], a[84:87], a[124:127], v[100:103]\n"
    "v_mfma_f32_16x16x32_bf16 v[100:103], a[4:7], a[104:107], v[100:103]\n"
    "v_mfma_f32_16x16x32_bf16 v[100:103], a[20:23], a[108:111], v[100:103]\n"
    "s_add_u32 m0, s50, 0\n"
    "v_mfma_f32_16x16x32_bf16 v[104:107], a[40:43], a[112:115], v[104:107]\n"
    "s_add_u32 s43, s48, 0\n"
    "v_mfma_f32_16x16x32_bf16 v[104:107], a[56:59], a[116:119], v[104:107]\n"
    "buffer_load_dwordx4 v12, s[60:63], s43 offen offset:0  lds\n"
    "v_mfma_f32_16x16x32_bf16 v[104:107], a[72:75], a[120:123], v[104:107]\n"
    "v_mfma_f32_16x16x32_bf16 v[104:107], a[88:91], a[124:127], v[104:107]\n"
    "v_mfma_f32_16x16x32_bf16 v[104:107], a[8:11], a[104:107], v[104:107]\n"
    "v_mfma_f32_16x16x32_bf16 v[104:107], a[24:27], a[108:111], v[104:107]\n"
    "v_mfma_f32_16x16x32_bf16 v[108:111], a[44:47], a[112:115], v[108:111]\n"
    "buffer_load_dwordx4 v13, s[60:63], s43 offen offset:1024  lds\n"
    "v_mfma_f32_16x16x32_bf16 v[108:111], a[60:63], a[116:119], v[108:111]\n"
    "v_mfma_f32_16x16x32_bf16 v[108:111], a[76:79], a[120:123], v[108:111]\n"
    "v_mfma_f32_16x16x32_bf16 v[108:111], a[92:95], a[124:127], v[108:111]\n"
    "v_mfma_f32_16x16x32_bf16 v[108:111], a[12:15], a[104:107], v[108:111]\n"
    "v_mfma_f32_16x16x32_bf16 v[108:111], a[28:31], a[108:111], v[108:111]\n"
    "s_add_u32 m0, s50, 32896\n"
    "v_mfma_f32_16x16x32_bf16 v[112:115], a[32:35], a[120:123], v[112:115]\n"
    "s_add_u32 s43, s48, 1048576\n"
    "v_mfma_f32_16x16x32_bf16 v[112:115], a[48:51], a[124:127], v[112:115]\n"
    "ds_read_b128 a[104:107], v2 offset:640\n"
    "s_waitcnt lgkmcnt(1)\n"
    "v_mfma_f32_16x16x32_bf16 v[112:115], a[64:67], a[96:99], v[112:115]\n"
    "buffer_load_dwordx4 v12, s[60:63], s43 offen offset:0  lds\n"
    "v_mfma_f32_16x16x32_bf16 v[112:115], a[80:83], a[100:103], v[112:115]\n"
    "ds_read_b128 a[108:111], v2 offset:704\n"
    "v_mfma_f32_16x16x32_bf16 v[112:115], a[0:3], a[112:115], v[112:115]\n"
    "v_mfma_f32_16x16x32_bf16 v[112:115], a[16:19], a[116:119], v[112:115]\n"
    "v_mfma_f32_16x16x32_bf16 v[116:119], a[36:39], a[120:123], v[116:119]\n"
    "v_mfma_f32_16x16x32_bf16 v[116:119], a[52:55], a[124:127], v[116:119]\n"
    "buffer_load_dwordx4 v13, s[60:63], s43 offen offset:1024  lds\n"
    "v_mfma_f32_16x16x32_bf16 v[116:119], a[68:71], a[96:99], v[116:119]\n"
    "v_mfma_f32_16x16x32_bf16 v[116:119], a[84:87], a[100:103], v[116:119]\n"
    "v_mfma_f32_16x16x32_bf16 v[116:119], a[4:7], a[112:115], v[116:119]\n"
    "v_mfma_f32_16x16x32_bf16 v[116:119], a[20:23], a[116:119], v[116:119]\n"
    "v_mfma_f32_16x16x32_bf16 v[120:123], a[40:43], a[120:123], v[120:123]\n"
    "s_add_u32 m0, s50, 24672\n"
    "v_mfma_f32_16x16x32_bf16 v[120:123], a[56:59], a[124:127], v[120:123]\n"
    "s_add_u32 s43, s48, 557056\n"
    "v_mfma_f32_16x16x32_bf16 v[120:123], a[72:75], a[96:99], v[120:123]\n"
    "buffer_load_dwordx4 v12, s[60:63], s43 offen offset:0  lds\n"
    "v_mfma_f32_16x16x32_bf16 v[120:123], a[88:91], a[100:103], v[120:123]\n"
    "v_mfma_f32_16x16x32_bf16 v[120:123], a[8:11], a[112:115], v[120:123]\n"
    "v_mfma_f32_16x16x32_bf16 v[120:123], a[24:27], a[116:119], v[120:123]\n"
    "v_mfma_f32_16x16x32_bf16 v[124:127], a[44:47], a[120:123], v[124:127]\n"
    "v_mfma_f32_16x16x32_bf16 v[124:127], a[60:63], a[124:127], v[124:127]\n"
    "buffer_load_dwordx4 v13, s[60:63], s43 offen offset:1024  lds\n"
    "v_mfma_f32_16x16x32_bf16 v[124:127], a[76:79], a[96:99], v[124:127]\n"
    "v_mfma_f32_16x16x32_bf16 v[124:127], a[92:95], a[100:103], v[124:127]\n"
    "v_mfma_f32_16x16x32_bf16 v[124:127], a[12:15], a[112:115], v[124:127]\n"
    "v_mfma_f32_16x16x32_bf16 v[124:127], a[28:31], a[116:119], v[124:127]\n"
    "v_mfma_f32_16x16x32_bf16 v[128:131], a[32:35], a[96:99], v[128:131]\n"
    "s_add_u32 m0, s50, 8224\n"
    "v_mfma_f32_16x16x32_bf16 v[128:131], a[48:51], a[100:103], v[128:131]\n"
    "ds_read_b128 a[112:115], v2 offset:768\n"
    "s_add_u32 s43, s48, 32768\n"
    "s_waitcnt lgkmcnt(1)\n"
    "v_mfma_f32_16x16x32_bf16 v[128:131], a[64:67], a[104:107], v[128:131]\n"
    "buffer_load_dwordx4 v12, s[60:63], s43 offen offset:0  lds\n"
    "v_mfma_f32_16x16x32_bf16 v[128:131], a[80:83], a[108:111], v[128:131]\n"
    "ds_read_b128 a[116:119], v2 offset:832\n"
    "v_mfma_f32_16x16x32_bf16 v[128:131], a[0:3], a[120:123], v[128:131]\n"
    "v_mfma_f32_16x16x32_bf16 v[128:131], a[16:19], a[124:127], v[128:131]\n"
    "v_mfma_f32_16x16x32_bf16 v[132:135], a[36:39], a[96:99], v[132:135]\n"
    "v_mfma_f32_16x16x32_bf16 v[132:135], a[52:55], a[100:103], v[132:135]\n"
    "buffer_load_dwordx4 v13, s[60:63], s43 offen offset:1024  lds\n"
    "v_mfma_f32_16x16x32_bf16 v[132:135], a[68:71], a[104:107], v[132:135]\n"
    "v_mfma_f32_16x16x32_bf16 v[132:135], a[84:87], a[108:111], v[132:135]\n"
    "v_mfma_f32_16x16x32_bf16 v[132:135], a[4:7], a[120:123], v[132:135]\n"
    "v_mfma_f32_16x16x32_bf16 v[132:135], a[20:23], a[124:127], v[132:135]\n"
    "v_mfma_f32_16x16x32_bf16 v[136:139], a[40:43], a[96:99], v[136:139]\n"
    "s_add_u32 m0, s50, 41120\n"
    "v_mfma_f32_16x16x32_bf16 v[136:139], a[56:59], a[100:103], v[136:139]\n"
    "s_add_u32 s43, s48, 1081344\n"
    "v_mfma_f32_16x16x32_bf16 v[136:139], a[72:75], a[104:107], v[136:139]\n"
    "buffer_load_dwordx4 v12, s[60:63], s43 offen offset:0  lds\n"
    "v_mfma_f32_16x16x32_bf16 v[136:139], a[88:91], a[108:111], v[136:139]\n"
    "v_mfma_f32_16x16x32_bf16 v[136:139], a[8:11], a[120:123], v[136:139]\n"
    "v_mfma_f32_16x16x32_bf16 v[136:139], a[24:27], a[124:127], v[136:139]\n"
    "v_mfma_f32_16x16x32_bf16 v[140:143], a[44:47], a[96:99], v[140:143]\n"
    "v_mfma_f32_16x16x32_bf16 v[140:143], a[60:63], a[100:103], v[140:143]\n"
    "buffer_load_dwordx4 v13, s[60:63], s43 offen offset:1024  lds\n"
    "v_mfma_f32_16x16x32_bf16 v[140:143], a[76:79], a[104:107], v[140:143]\n"
    "v_mfma_f32_16x16x32_bf16 v[140:143], a[92:95], a[108:111], v[140:143]\n"
    "v_mfma_f32_16x16x32_bf16 v[140:143], a[12:15], a[120:123], v[140:143]\n"
    "v_mfma_f32_16x16x32_bf16 v[140:143], a[28:31], a[124:127], v[140:143]\n"
    "v_mfma_f32_16x16x32_bf16 v[144:147], a[32:35], a[104:107], v[144:147]\n"
    "v_mfma_f32_16x16x32_bf16 v[144:147], a[48:51], a[108:111], v[144:147]\n"
    "ds_read_b128 a[120:123], v2 offset:896\n"
    "s_waitcnt lgkmcnt(1)\n"
    "v_mfma_f32_16x16x32_bf16 v[144:147], a[64:67], a[112:115], v[144:147]\n"
    "v_mfma_f32_16x16x32_bf16 v[144:147], a[80:83], a[116:119], v[144:147]\n"
    "ds_read_b128 a[124:127], v2 offset:960\n"
    "v_mfma_f32_16x16x32_bf16 v[144:147], a[0:3], a[96:99], v[144:147]\n"
    "v_mfma_f32_16x16x32_bf16 v[144:147], a[16:19], a[100:103], v[144:147]\n"
    "v_mfma_f32_16x16x32_bf16 v[148:151], a[36:39], a[104:107], v[148:151]\n"
    "v_mfma_f32_16x16x32_bf16 v[148:151], a[52:55], a[108:111], v[148:151]\n"
    "v_mfma_f32_16x16x32_bf16 v[148:151], a[68:71], a[112:115], v[148:151]\n"
    "v_mfma_f32_16x16x32_bf16 v[148:151], a[84:87], a[116:119], v[148:151]\n"
    "v_mfma_f32_16x16x32_bf16 v[148:151], a[4:7], a[96:99], v[148:151]\n"
    "v_mfma_f32_16x16x32_bf16 v[148:151], a[20:23], a[100:103], v[148:151]\n"
    "v_mfma_f32_16x16x32_bf16 v[152:155], a[40:43], a[104:107], v[152:155]\n"
    "v_mfma_f32_16x16x32_bf16 v[152:155], a[56:59], a[108:111], v[152:155]\n"
    "v_mfma_f32_16x16x32_bf16 v[152:155], a[72:75], a[112:115], v[152:155]\n"
    "v_mfma_f32_16x16x32_bf16 v[152:155], a[88:91], a[116:119], v[152:155]\n"
    "v_mfma_f32_16x16x32_bf16 v[152:155], a[8:11], a[96:99], v[152:155]\n"
    "v_mfma_f32_16x16x32_bf16 v[152:155], a[24:27], a[100:103], v[152:155]\n"
    "v_mfma_f32_16x16x32_bf16 v[156:159], a[44:47], a[104:107], v[156:159]\n"
    "v_mfma_f32_16x16x32_bf16 v[156:159], a[60:63], a[108:111], v[156:159]\n"
    "v_mfma_f32_16x16x32_bf16 v[156:159], a[76:79], a[112:115], v[156:159]\n"
    "v_mfma_f32_16x16x32_bf16 v[156:159], a[92:95], a[116:119], v[156:159]\n"
    "v_mfma_f32_16x16x32_bf16 v[156:159], a[12:15], a[96:99], v[156:159]\n"
    "v_mfma_f32_16x16x32_bf16 v[156:159], a[28:31], a[100:103], v[156:159]\n"
    "s_waitcnt vmcnt(20) lgkmcnt(0)\n"
    "s_barrier\n"
    "v_add_u32 v2, s41, v4\n"
    "v_mfma_f32_16x16x32_bf16 v[160:163], a[32:35], a[112:115], v[160:163]\n"
    "v_mfma_f32_16x16x32_bf16 v[160:163], a[48:51], a[116:119], v[160:163]\n"
    "ds_read_b128 a[96:99], v2 offset:0\n"
    "v_mfma_f32_16x16x32_bf16 v[160:163], a[64:67], a[120:123], v[160:163]\n"
    "v_mfma_f32_16x16x32_bf16 v[160:163], a[80:83], a[124:127], v[160:163]\n"
    "ds_read_b128 a[100:103], v2 offset:64\n"
    "v_add_u32 v3, s41, v5\n"
    "v_mfma_f32_16x16x32_bf16 v[160:163], a[0:3], a[104:107], v[160:163]\n"
    "s_add_u32 m0, s40, s49\n"
    "v_mfma_f32_16x16x32_bf16 v[160:163], a[16:19], a[108:111], v[160:163]\n"
    "s_add_u32 s43, s47, 0\n"
    "v_mfma_f32_16x16x32_bf16 v[164:167], a[36:39], a[112:115], v[164:167]\n"
    "buffer_load_dwordx4 v14, s[56:59], s43 offen offset:0  lds\n"
    "v_mfma_f32_16x16x32_bf16 v[164:167], a[52:55], a[116:119], v[164:167]\n"
    "v_mfma_f32_16x16x32_bf16 v[164:167], a[68:71], a[120:123], v[164:167]\n"
    "v_mfma_f32_16x16x32_bf16 v[164:167], a[84:87], a[124:127], v[164:167]\n"
    "v_mfma_f32_16x16x32_bf16 v[164:167], a[4:7], a[104:107], v[164:167]\n"
    "v_mfma_f32_16x16x32_bf16 v[164:167], a[20:23], a[108:111], v[164:167]\n"
    "v_mfma_f32_16x16x32_bf16 v[168:171], a[40:43], a[112:115], v[168:171]\n"
    "v_mfma_f32_16x16x32_bf16 v[168:171], a[56:59], a[116:119], v[168:171]\n"
    "v_mfma_f32_16x16x32_bf16 v[168:171], a[72:75], a[120:123], v[168:171]\n"
    "buffer_load_dwordx4 v15, s[56:59], s43 offen offset:1056  lds\n"
    "v_mfma_f32_16x16x32_bf16 v[168:171], a[88:91], a[124:127], v[168:171]\n"
    "v_mfma_f32_16x16x32_bf16 v[168:171], a[8:11], a[104:107], v[168:171]\n"
    "v_mfma_f32_16x16x32_bf16 v[168:171], a[24:27], a[108:111], v[168:171]\n"
    "v_mfma_f32_16x16x32_bf16 v[172:175], a[44:47], a[112:115], v[172:175]\n"
    "v_mfma_f32_16x16x32_bf16 v[172:175], a[60:63], a[116:119], v[172:175]\n"
    "v_mfma_f32_16x16x32_bf16 v[172:175], a[76:79], a[120:123], v[172:175]\n"
    "v_mfma_f32_16x16x32_bf16 v[172:175], a[92:95], a[124:127], v[172:175]\n"
    "v_mfma_f32_16x16x32_bf16 v[172:175], a[12:15], a[104:107], v[172:175]\n"
    "buffer_load_dwordx4 v16, s[56:59], s43 offen offset:2112  lds\n"
    "v_mfma_f32_16x16x32_bf16 v[172:175], a[28:31], a[108:111], v[172:175]\n"
    "v_mfma_f32_16x16x32_bf16 v[176:179], a[32:35], a[120:123], v[176:179]\n"
    "v_mfma_f32_16x16x32_bf16 v[176:179], a[48:51], a[124:127], v[176:179]\n"
    "ds_read_b128 a[104:107], v2 offset:128\n"
    "s_waitcnt lgkmcnt(1)\n"
    "v_mfma_f32_16x16x32_bf16 v[176:179], a[64:67], a[96:99], v[176:179]\n"
    "v_mfma_f32_16x16x32_bf16 v[176:179], a[80:83], a[100:103], v[176:179]\n"
    "ds_read_b128 a[108:111], v2 offset:192\n"
    "v_mfma_f32_16x16x32_bf16 v[176:179], a[0:3], a[112:115], v[176:179]\n"
    "v_mfma_f32_16x16x32_bf16 v[176:179], a[16:19], a[116:119], v[176:179]\n"
    "v_mfma_f32_16x16x32_bf16 v[180:183], a[36:39], a[120:123], v[180:183]\n"
    "buffer_load_dwordx4 v17, s[56:59], s43 offen offset:3168  lds\n"
    "v_mfma_f32_16x16x32_bf16 v[180:183], a[52:55], a[124:127], v[180:183]\n"
    "v_mfma_f32_16x16x32_bf16 v[180:183], a[68:71], a[96:99], v[180:183]\n"
    "v_mfma_f32_16x16x32_bf16 v[180:183], a[84:87], a[100:103], v[180:183]\n"
    "v_mfma_f32_16x16x32_bf16 v[180:183], a[4:7], a[112:115], v[180:183]\n"
    "v_mfma_f32_16x16x32_bf16 v[180:183], a[20:23], a[116:119], v[180:183]\n"
    "v_mfma_f32_16x16x32_bf16 v[184:187], a[40:43], a[120:123], v[184:187]\n"
    "v_mfma_f32_16x16x32_bf16 v[184:187], a[56:59], a[124:127], v[184:187]\n"
    "v_mfma_f32_16x16x32_bf16 v[184:187], a[72:75], a[96:99], v[184:187]\n"
    "s_add_u32 m0, m0, 4224\n"
    "v_mfma_f32_16x16x32_bf16 v[184:187], a[88:91], a[100:103], v[184:187]\n"
    "s_add_u32 s43, s47, 131072\n"
    "v_mfma_f32_16x16x32_bf16 v[184:187], a[8:11], a[112:115], v[184:187]\n"
    "buffer_load_dwordx4 v14, s[56:59], s43 offen offset:0  lds\n"
    "v_mfma_f32_16x16x32_bf16 v[184:187], a[24:27], a[116:119], v[184:187]\n"
    "v_mfma_f32_16x16x32_bf16 v[188:191], a[44:47], a[120:123], v[188:191]\n"
    "v_mfma_f32_16x16x32_bf16 v[188:191], a[60:63], a[124:127], v[188:191]\n"
    "v_mfma_f32_16x16x32_bf16 v[188:191], a[76:79], a[96:99], v[188:191]\n"
    "v_mfma_f32_16x16x32_bf16 v[188:191], a[92:95], a[100:103], v[188:191]\n"
    "v_mfma_f32_16x16x32_bf16 v[188:191], a[12:15], a[112:115], v[188:191]\n"
    "v_mfma_f32_16x16x32_bf16 v[188:191], a[28:31], a[116:119], v[188:191]\n"
    "v_mfma_f32_16x16x32_bf16 v[192:195], a[32:35], a[96:99], v[192:195]\n"
    "buffer_load_dwordx4 v15, s[56:59], s43 offen offset:1056  lds\n"
    "v_mfma_f32_16x16x32_bf16 v[192:195], a[48:51], a[100:103], v[192:195]\n"
    "ds_read_b128 a[112:115], v2 offset:256\n"
    "s_waitcnt lgkmcnt(1)\n"
    "v_mfma_f32_16x16x32_bf16 v[192:195], a[64:67], a[104:107], v[192:195]\n"
    "v_mfma_f32_16x16x32_bf16 v[192:195], a[80:83], a[108:111], v[192:195]\n"
    "ds_read_b128 a[116:119], v2 offset:320\n"
    "v_mfma_f32_16x16x32_bf16 v[192:195], a[0:3], a[120:123], v[192:195]\n"
    "v_mfma_f32_16x16x32_bf16 v[192:195], a[16:19], a[124:127], v[192:195]\n"
    "v_mfma_f32_16x16x32_bf16 v[196:199], a[36:39], a[96:99], v[196:199]\n"
    "v_mfma_f32_16x16x32_bf16 v[196:199], a[52:55], a[100:103], v[196:199]\n"
    "v_mfma_f32_16x16x32_bf16 v[196:199], a[68:71], a[104:107], v[196:199]\n"
    "buffer_load_dwordx4 v16, s[56:59], s43 offen offset:2112  lds\n"
    "v_mfma_f32_16x16x32_bf16 v[196:199], a[84:87], a[108:111], v[196:199]\n"
    "v_mfma_f32_16x16x32_bf16 v[196:199], a[4:7], a[120:123], v[196:199]\n"
    "v_mfma_f32_16x16x32_bf16 v[196:199], a[20:23], a[124:127], v[196:199]\n"
    "v_mfma_f32_16x16x32_bf16 v[200:203], a[40:43], a[96:99], v[200:203]\n"
    "v_mfma_f32_16x16x32_bf16 v[200:203], a[56:59], a[100:103], v[200:203]\n"
    "v_mfma_f32_16x16x32_bf16 v[200:203], a[72:75], a[104:107], v[200:203]\n"
    "v_mfma_f32_16x16x32_bf16 v[200:203], a[88:91], a[108:111], v[200:203]\n"
    "v_mfma_f32_16x16x32_bf16 v[200:203], a[8:11], a[120:123], v[200:203]\n"
    "buffer_load_dwordx4 v17, s[56:59], s43 offen offset:3168  lds\n"
    "v_mfma_f32_16x16x32_bf16 v[200:203], a[24:27], a[124:127], v[200:203]\n"
    "v_mfma_f32_16x16x32_bf16 v[204:207], a[44:47], a[96:99], v[204:207]\n"
    "v_mfma_f32_16x16x32_bf16 v[204:207], a[60:63], a[100:103], v[204:207]\n"
    "v_mfma_f32_16x16x32_bf16 v[204:207], a[76:79], a[104:107], v[204:207]\n"
    "v_mfma_f32_16x16x32_bf16 v[204:207], a[92:95], a[108:111], v[204:207]\n"
    "v_mfma_f32_16x16x32_bf16 v[204:207], a[12:15], a[120:123], v[204:207]\n"
    "v_mfma_f32_16x16x32_bf16 v[204:207], a[28:31], a[124:127], v[204:207]\n"
    "v_mfma_f32_16x16x32_bf16 v[208:211], a[32:35], a[104:107], v[208:211]\n"
    "v_mfma_f32_16x16x32_bf16 v[208:211], a[48:51], a[108:111], v[208:211]\n"
    "ds_read_b128 a[120:123], v2 offset:384\n"
    "s_waitcnt lgkmcnt(1)\n"
    "v_mfma_f32_16x16x32_bf16 v[208:211], a[64:67], a[112:115], v[208:211]\n"
    "v_mfma_f32_16x16x32_bf16 v[208:211], a[80:83], a[116:119], v[208:211]\n"
    "ds_read_b128 a[124:127], v2 offset:448\n"
    "v_mfma_f32_16x16x32_bf16 v[208:211], a[0:3], a[96:99], v[208:211]\n"
    "v_mfma_f32_16x16x32_bf16 v[208:211], a[16:19], a[100:103], v[208:211]\n"
    "v_mfma_f32_16x16x32_bf16 v[212:215], a[36:39], a[104:107], v[212:215]\n"
    "v_mfma_f32_16x16x32_bf16 v[212:215], a[52:55], a[108:111], v[212:215]\n"
    "v_mfma_f32_16x16x32_bf16 v[212:215], a[68:71], a[112:115], v[212:215]\n"
    "v_mfma_f32_16x16x32_bf16 v[212:215], a[84:87], a[116:119], v[212:215]\n"
    "v_mfma_f32_16x16x32_bf16 v[212:215], a[4:7], a[96:99], v[212:215]\n"
    "v_mfma_f32_16x16x32_bf16 v[212:215], a[20:23], a[100:103], v[212:215]\n"
    "v_mfma_f32_16x16x32_bf16 v[216:219], a[40:43], a[104:107], v[216:219]\n"
    "v_mfma_f32_16x16x32_bf16 v[216:219], a[56:59], a[108:111], v[216:219]\n"
    "v_mfma_f32_16x16x32_bf16 v[216:219], a[72:75], a[112:115], v[216:219]\n"
    "v_mfma_f32_16x16x32_bf16 v[216:219], a[88:91], a[116:119], v[216:219]\n"
    "v_mfma_f32_16x16x32_bf16 v[216:219], a[8:11], a[96:99], v[216:219]\n"
    "v_mfma_f32_16x16x32_bf16 v[216:219], a[24:27], a[100:103], v[216:219]\n"
    "v_mfma_f32_16x16x32_bf16 v[220:223], a[44:47], a[104:107], v[220:223]\n"
    "v_mfma_f32_16x16x32_bf16 v[220:223], a[60:63], a[108:111], v[220:223]\n"
    "v_mfma_f32_16x16x32_bf16 v[220:223], a[76:79], a[112:115], v[220:223]\n"
    "v_mfma_f32_16x16x32_bf16 v[220:223], a[92:95], a[116:119], v[220:223]\n"
    "v_mfma_f32_16x16x32_bf16 v[220:223], a[12:15], a[96:99], v[220:223]\n"
    "v_mfma_f32_16x16x32_bf16 v[220:223], a[28:31], a[100:103], v[220:223]\n"
    "v_mfma_f32_16x16x32_bf16 v[224:227], a[32:35], a[112:115], v[224:227]\n"
    "v_mfma_f32_16x16x32_bf16 v[224:227], a[48:51], a[116:119], v[224:227]\n"
    "ds_read_b128 a[96:99], v2 offset:512\n"
    "s_waitcnt lgkmcnt(1)\n"
    "v_mfma_f32_16x16x32_bf16 v[224:227], a[64:67], a[120:123], v[224:227]\n"
    "v_mfma_f32_16x16x32_bf16 v[224:227], a[80:83], a[124:127], v[224:227]\n"
    "ds_read_b128 a[100:103], v2 offset:576\n"
    "v_mfma_f32_16x16x32_bf16 v[224:227], a[0:3], a[104:107], v[224:227]\n"
    "v_mfma_f32_16x16x32_bf16 v[224:227], a[16:19], a[108:111], v[224:227]\n"
    "v_mfma_f32_16x16x32_bf16 v[228:231], a[36:39], a[112:115], v[228:231]\n"
    "v_mfma_f32_16x16x32_bf16 v[228:231], a[52:55], a[116:119], v[228:231]\n"
    "v_mfma_f32_16x16x32_bf16 v[228:231], a[68:71], a[120:123], v[228:231]\n"
    "v_mfma_f32_16x16x32_bf16 v[228:231], a[84:87], a[124:127], v[228:231]\n"
    "v_mfma_f32_16x16x32_bf16 v[228:231], a[4:7], a[104:107], v[228:231]\n"
    "v_mfma_f32_16x16x32_bf16 v[228:231], a[20:23], a[108:111], v[228:231]\n"
    "v_mfma_f32_16x16x32_bf16 v[232:235], a[40:43], a[112:115], v[232:235]\n"
    "v_mfma_f32_16x16x32_bf16 v[232:235], a[56:59], a[116:119], v[232:235]\n"
    "v_mfma_f32_16x16x32_bf16 v[232:235], a[72:75], a[120:123], v[232:235]\n"
    "v_mfma_f32_16x16x32_bf16 v[232:235], a[88:91], a[124:127], v[232:235]\n"
    "v_mfma_f32_16x16x32_bf16 v[232:235], a[8:11], a[104:107], v[232:235]\n"
    "v_mfma_f32_16x16x32_bf16 v[232:235], a[24:27], a[108:111], v[232:235]\n"
    "v_mfma_f32_16x16x32_bf16 v[236:239], a[44:47], a[112:115], v[236:239]\n"
    "v_mfma_f32_16x16x32_bf16 v[236:239], a[60:63], a[116:119], v[236:239]\n"
    "v_mfma_f32_16x16x32_bf16 v[236:239], a[76:79], a[120:123], v[236:239]\n"
    "v_mfma_f32_16x16x32_bf16 v[236:239], a[92:95], a[124:127], v[236:239]\n"
    "v_mfma_f32_16x16x32_bf16 v[236:239], a[12:15], a[104:107], v[236:239]\n"
    "v_mfma_f32_16x16x32_bf16 v[236:239], a[28:31], a[108:111], v[236:239]\n"
    "v_mfma_f32_16x16x32_bf16 v[240:243], a[32:35], a[120:123], v[240:243]\n"
    "v_mfma_f32_16x16x32_bf16 v[240:243], a[48:51], a[124:127], v[240:243]\n"
    "ds_read_b128 a[104:107], v2 offset:640\n"
    "s_waitcnt lgkmcnt(1)\n"
    "v_mfma_f32_16x16x32_bf16 v[240:243], a[64:67], a[96:99], v[240:243]\n"
    "v_mfma_f32_16x16x32_bf16 v[240:243], a[80:83], a[100:103], v[240:243]\n"
    "ds_read_b128 a[108:111], v2 offset:704\n"
    "v_mfma_f32_16x16x32_bf16 v[240:243], a[0:3], a[112:115], v[240:243]\n"
    "v_mfma_f32_16x16x32_bf16 v[240:243], a[16:19], a[116:119], v[240:243]\n"
    "v_mfma_f32_16x16x32_bf16 v[244:247], a[36:39], a[120:123], v[244:247]\n"
    "v_mfma_f32_16x16x32_bf16 v[244:247], a[52:55], a[124:127], v[244:247]\n"
    "v_mfma_f32_16x16x32_bf16 v[244:247], a[68:71], a[96:99], v[244:247]\n"
    "v_mfma_f32_16x16x32_bf16 v[244:247], a[84:87], a[100:103], v[244:247]\n"
    "v_mfma_f32_16x16x32_bf16 v[244:247], a[4:7], a[112:115], v[244:247]\n"
    "v_mfma_f32_16x16x32_bf16 v[244:247], a[20:23], a[116:119], v[244:247]\n"
    "v_mfma_f32_16x16x32_bf16 v[248:251], a[40:43], a[120:123], v[248:251]\n"
    "v_mfma_f32_16x16x32_bf16 v[248:251], a[56:59], a[124:127], v[248:251]\n"
    "v_mfma_f32_16x16x32_bf16 v[248:251], a[72:75], a[96:99], v[248:251]\n"
    "v_mfma_f32_16x16x32_bf16 v[248:251], a[88:91], a[100:103], v[248:251]\n"
    "v_mfma_f32_16x16x32_bf16 v[248:251], a[8:11], a[112:115], v[248:251]\n"
    "v_mfma_f32_16x16x32_bf16 v[248:251], a[24:27], a[116:119], v[248:251]\n"
    "v_mfma_f32_16x16x32_bf16 v[252:255], a[44:47], a[120:123], v[252:255]\n"
    "v_mfma_f32_16x16x32_bf16 v[252:255], a[60:63], a[124:127], v[252:255]\n"
    "v_mfma_f32_16x16x32_bf16 v[252:255], a[76:79], a[96:99], v[252:255]\n"
    "v_mfma_f32_16x16x32_bf16 v[252:255], a[92:95], a[100:103], v[252:255]\n"
    "v_mfma_f32_16x16x32_bf16 v[252:255], a[12:15], a[112:115], v[252:255]\n"
    "v_mfma_f32_16x16x32_bf16 v[252:255], a[28:31], a[116:119], v[252:255]\n"
    "v_mfma_f32_16x16x32_bf16 a[136:139], a[32:35], a[96:99], a[136:139]\n"
    "v_mfma_f32_16x16x32_bf16 a[136:139], a[48:51], a[100:103], a[136:139]\n"
    "ds_read_b128 a[112:115], v2 offset:768\n"
    "s_waitcnt lgkmcnt(1)\n"
    "v_mfma_f32_16x16x32_bf16 a[136:139], a[64:67], a[104:107], a[136:139]\n"
    "v_mfma_f32_16x16x32_bf16 a[136:139], a[80:83], a[108:111], a[136:139]\n"
    "ds_read_b128 a[116:119], v2 offset:832\n"
    "v_mfma_f32_16x16x32_bf16 a[136:139], a[0:3], a[120:123], a[136:139]\n"
    "v_mfma_f32_16x16x32_bf16 a[136:139], a[16:19], a[124:127], a[136:139]\n"
    "v_mfma_f32_16x16x32_bf16 a[140:143], a[36:39], a[96:99], a[140:143]\n"
    "v_mfma_f32_16x16x32_bf16 a[140:143], a[52:55], a[100:103], a[140:143]\n"
    "v_mfma_f32_16x16x32_bf16 a[140:143], a[68:71], a[104:107], a[140:143]\n"
    "v_mfma_f32_16x16x32_bf16 a[140:143], a[84:87], a[108:111], a[140:143]\n"
    "v_mfma_f32_16x16x32_bf16 a[140:143], a[4:7], a[120:123], a[140:143]\n"
    "v_mfma_f32_16x16x32_bf16 a[140:143], a[20:23], a[124:127], a[140:143]\n"
    "v_mfma_f32_16x16x32_bf16 a[144:147], a[40:43], a[96:99], a[144:147]\n"
    "v_mfma_f32_16x16x32_bf16 a[144:147], a[56:59], a[100:103], a[144:147]\n"
    "v_mfma_f32_16x16x32_bf16 a[144:147], a[72:75], a[104:107], a[144:147]\n"
    "v_mfma_f32_16x16x32_bf16 a[144:147], a[88:91], a[108:111], a[144:147]\n"
    "v_mfma_f32_16x16x32_bf16 a[144:147], a[8:11], a[120:123], a[144:147]\n"
    "v_mfma_f32_16x16x32_bf16 a[144:147], a[24:27], a[124:127], a[144:147]\n"
    "v_mfma_f32_16x16x32_bf16 a[148:151], a[44:47], a[96:99], a[148:151]\n"
    "v_mfma_f32_16x16x32_bf16 a[148:151], a[60:63], a[100:103], a[148:151]\n"
    "v_mfma_f32_16x16x32_bf16 a[148:151], a[76:79], a[104:107], a[148:151]\n"
    "v_mfma_f32_16x16x32_bf16 a[148:151], a[92:95], a[108:111], a[148:151]\n"
    "v_mfma_f32_16x16x32_bf16 a[148:151], a[12:15], a[120:123], a[148:151]\n"
    "v_mfma_f32_16x16x32_bf16 a[148:151], a[28:31], a[124:127], a[148:151]\n"
    "v_mfma_f32_16x16x32_bf16 a[152:155], a[32:35], a[104:107], a[152:155]\n"
    "v_mfma_f32_16x16x32_bf16 a[152:155], a[48:51], a[108:111], a[152:155]\n"
    "ds_read_b128 a[120:123], v2 offset:896\n"
    "s_waitcnt lgkmcnt(1)\n"
    "v_mfma_f32_16x16x32_bf16 a[152:155], a[64:67], a[112:115], a[152:155]\n"
    "v_mfma_f32_16x16x32_bf16 a[152:155], a[80:83], a[116:119], a[152:155]\n"
    "ds_read_b128 a[124:127], v2 offset:960\n"
    "v_mfma_f32_16x16x32_bf16 a[152:155], a[0:3], a[96:99], a[152:155]\n"
    "v_mfma_f32_16x16x32_bf16 a[152:155], a[16:19], a[100:103], a[152:155]\n"
    "v_mfma_f32_16x16x32_bf16 a[156:159], a[36:39], a[104:107], a[156:159]\n"
    "v_mfma_f32_16x16x32_bf16 a[156:159], a[52:55], a[108:111], a[156:159]\n"
    "s_mov_b32 s44, s40\n"
    "v_mfma_f32_16x16x32_bf16 a[156:159], a[68:71], a[112:115], a[156:159]\n"
    "v_mfma_f32_16x16x32_bf16 a[156:159], a[84:87], a[116:119], a[156:159]\n"
    "s_mov_b32 s40, s42\n"
    "v_mfma_f32_16x16x32_bf16 a[156:159], a[4:7], a[96:99], a[156:159]\n"
    "v_mfma_f32_16x16x32_bf16 a[156:159], a[20:23], a[100:103], a[156:159]\n"
    "s_mov_b32 s42, s41\n"
    "v_mfma_f32_16x16x32_bf16 a[160:163], a[40:43], a[104:107], a[160:163]\n"
    "v_mfma_f32_16x16x32_bf16 a[160:163], a[56:59], a[108:111], a[160:163]\n"
    "ds_read_b128 a[128:131], v3 offset:896\n"
    "s_mov_b32 s41, s44\n"
    "v_mfma_f32_16x16x32_bf16 a[160:163], a[72:75], a[112:115], a[160:163]\n"
    "v_mfma_f32_16x16x32_bf16 a[160:163], a[88:91], a[116:119], a[160:163]\n"
    "ds_read_b128 a[132:135], v3 offset:960\n"
    "s_add_u32 s46, s46, 128\n"
    "v_mfma_f32_16x16x32_bf16 a[160:163], a[8:11], a[96:99], a[160:163]\n"
    "v_mfma_f32_16x16x32_bf16 a[160:163], a[24:27], a[100:103], a[160:163]\n"
    "s_add_u32 s47, s47, 128\n"
    "v_mfma_f32_16x16x32_bf16 a[164:167], a[44:47], a[104:107], a[164:167]\n"
    "v_mfma_f32_16x16x32_bf16 a[164:167], a[60:63], a[108:111], a[164:167]\n"
    "s_add_u32 s48, s48, 65536\n"
    "v_mfma_f32_16x16x32_bf16 a[164:167], a[76:79], a[112:115], a[164:167]\n"
    "v_mfma_f32_16x16x32_bf16 a[164:167], a[92:95], a[116:119], a[164:167]\n"
    "s_add_u32 s45, s45, 1\n"
    "v_mfma_f32_16x16x32_bf16 a[164:167], a[12:15], a[96:99], a[164:167]\n"
    "v_mfma_f32_16x16x32_bf16 a[164:167], a[28:31], a[100:103], a[164:167]\n"
    "s_cmp_lt_u32 s45, 7\n"
    "v_mfma_f32_16x16x32_bf16 a[168:171], a[32:35], a[112:115], a[168:171]\n"
    "v_mfma_f32_16x16x32_bf16 a[168:171], a[48:51], a[116:119], a[168:171]\n"
    "s_waitcnt lgkmcnt(0)\n"
    "v_mfma_f32_16x16x32_bf16 a[168:171], a[64:67], a[120:123], a[168:171]\n"
    "v_mfma_f32_16x16x32_bf16 a[168:171], a[80:83], a[124:127], a[168:171]\n"
    "v_mfma_f32_16x16x32_bf16 a[168:171], a[0:3], a[104:107], a[168:171]\n"
    "v_mfma_f32_16x16x32_bf16 a[168:171], a[16:19], a[108:111], a[168:171]\n"
    "v_mfma_f32_16x16x32_bf16 a[172:175], a[36:39], a[112:115], a[172:175]\n"
    "v_mfma_f32_16x16x32_bf16 a[172:175], a[52:55], a[116:119], a[172:175]\n"
    "v_mfma_f32_16x16x32_bf16 a[172:175], a[68:71], a[120:123], a[172:175]\n"
    "v_mfma_f32_16x16x32_bf16 a[172:175], a[84:87], a[124:127], a[172:175]\n"
    "v_mfma_f32_16x16x32_bf16 a[172:175], a[4:7], a[104:107], a[172:175]\n"
    "v_mfma_f32_16x16x32_bf16 a[172:175], a[20:23], a[108:111], a[172:175]\n"
    "v_mfma_f32_16x16x32_bf16 a[176:179], a[40:43], a[112:115], a[176:179]\n"
    "v_mfma_f32_16x16x32_bf16 a[176:179], a[56:59], a[116:119], a[176:179]\n"
    "v_mfma_f32_16x16x32_bf16 a[176:179], a[72:75], a[120:123], a[176:179]\n"
    "v_mfma_f32_16x16x32_bf16 a[176:179], a[88:91], a[124:127], a[176:179]\n"
    "v_mfma_f32_16x16x32_bf16 a[176:179], a[8:11], a[104:107], a[176:179]\n"
    "v_mfma_f32_16x16x32_bf16 a[176:179], a[24:27], a[108:111], a[176:179]\n"
    "v_mfma_f32_16x16x32_bf16 a[180:183], a[44:47], a[112:115], a[180:183]\n"
    "v_mfma_f32_16x16x32_bf16 a[180:183], a[60:63], a[116:119], a[180:183]\n"
    "v_mfma_f32_16x16x32_bf16 a[180:183], a[76:79], a[120:123], a[180:183]\n"
    "v_mfma_f32_16x16x32_bf16 a[180:183], a[92:95], a[124:127], a[180:183]\n"
    "v_mfma_f32_16x16x32_bf16 a[180:183], a[12:15], a[104:107], a[180:183]\n"
    "v_mfma_f32_16x16x32_bf16 a[180:183], a[28:31], a[108:111], a[180:183]\n"
    "s_waitcnt vmcnt(8) lgkmcnt(0)\n"
    "s_barrier\n"
    "v_add_u32 v2, s40, v4\n"
    "v_mfma_f32_16x16x32_bf16 a[184:187], a[32:35], a[120:123], a[184:187]\n"
    "ds_read_b128 a[96:99], v2 offset:0\n"
    "ds_read_b128 a[100:103], v2 offset:64\n"
    "v_mfma_f32_16x16x32_bf16 a[184:187], a[48:51], a[124:127], a[184:187]\n"
    "ds_read_b64_tr_b16 a[32:33], v10 offset:0\n"
    "v_mfma_f32_16x16x32_bf16 a[184:187], a[64:67], a[128:131], a[184:187]\n"
    "ds_read_b64_tr_b16 a[34:35], v10 offset:1024\n"
    "v_mfma_f32_16x16x32_bf16 a[184:187], a[80:83], a[132:135], a[184:187]\n"
    "ds_read_b64_tr_b16 a[48:49], v10 offset:8224\n"
    "ds_read_b64_tr_b16 a[50:51], v10 offset:9248\n"
    "v_mfma_f32_16x16x32_bf16 a[184:187], a[0:3], a[112:115], a[184:187]\n"
    "ds_read_b64_tr_b16 a[64:65], v8 offset:0\n"
    "v_mfma_f32_16x16x32_bf16 a[184:187], a[16:19], a[116:119], a[184:187]\n"
    "ds_read_b64_tr_b16 a[66:67], v8 offset:1024\n"
    "v_mfma_f32_16x16x32_bf16 a[188:191], a[36:39], a[120:123], a[188:191]\n"
    "ds_read_b128 a[104:107], v2 offset:128\n"
    "ds_read_b64_tr_b16 a[80:81], v8 offset:8224\n"
    "v_mfma_f32_16x16x32_bf16 a[188:191], a[52:55], a[124:127], a[188:191]\n"
    "ds_read_b64_tr_b16 a[82:83], v8 offset:9248\n"
    "v_mfma_f32_16x16x32_bf16 a[188:191], a[68:71], a[128:131], a[188:191]\n"
    "ds_read_b128 a[108:111], v2 offset:192\n"
    "v_mfma_f32_16x16x32_bf16 a[188:191], a[84:87], a[132:135], a[188:191]\n"
    "ds_read_b64_tr_b16 a[36:37], v10 offset:8\n"
    "ds_read_b64_tr_b16 a[38:39], v10 offset:1032\n"
    "v_mfma_f32_16x16x32_bf16 a[188:191], a[4:7], a[112:115], a[188:191]\n"
    "ds_read_b64_tr_b16 a[52:53], v10 offset:8232\n"
    "v_mfma_f32_16x16x32_bf16 a[188:191], a[20:23], a[116:119], a[188:191]\n"
    "ds_read_b64_tr_b16 a[54:55], v10 offset:9256\n"
    "v_mfma_f32_16x16x32_bf16 a[192:195], a[40:43], a[120:123], a[192:195]\n"
    "ds_read_b64_tr_b16 a[68:69], v8 offset:8\n"
    "ds_read_b64_tr_b16 a[70:71], v8 offset:1032\n"
    "v_mfma_f32_16x16x32_bf16 a[192:195], a[56:59], a[124:127], a[192:195]\n"
    "ds_read_b64_tr_b16 a[84:85], v8 offset:8232\n"
    "v_mfma_f32_16x16x32_bf16 a[192:195], a[72:75], a[128:131], a[192:195]\n"
    "ds_read_b64_tr_b16 a[86:87], v8 offset:9256\n"
    "v_mfma_f32_16x16x32_bf16 a[192:195], a[88:91], a[132:135], a[192:195]\n"
    "ds_read_b64_tr_b16 a[40:41], v11 offset:0\n"
    "ds_read_b64_tr_b16 a[42:43], v11 offset:1024\n"
    "v_mfma_f32_16x16x32_bf16 a[192:195], a[8:11], a[112:115], a[192:195]\n"
    "ds_read_b64_tr_b16 a[56:57], v11 offset:8224\n"
    "v_mfma_f32_16x16x32_bf16 a[192:195], a[24:27], a[116:119], a[192:195]\n"
    "ds_read_b64_tr_b16 a[58:59], v11 offset:9248\n"
    "v_mfma_f32_16x16x32_bf16 a[196:199], a[44:47], a[120:123], a[196:199]\n"
    "ds_read_b64_tr_b16 a[72:73], v9 offset:0\n"
    "ds_read_b64_tr_b16 a[74:75], v9 offset:1024\n"
    "v_mfma_f32_16x16x32_bf16 a[196:199], a[60:63], a[124:127], a[196:199]\n"
    "ds_read_b64_tr_b16 a[88:89], v9 offset:8224\n"
    "v_mfma_f32_16x16x32_bf16 a[196:199], a[76:79], a[128:131], a[196:199]\n"
    "ds_read_b64_tr_b16 a[90:91], v9 offset:9248\n"
    "v_mfma_f32_16x16x32_bf16 a[196:199], a[92:95], a[132:135], a[196:199]\n"
    "ds_read_b64_tr_b16 a[44:45], v11 offset:8\n"
    "ds_read_b64_tr_b16 a[46:47], v11 offset:1032\n"
    "v_mfma_f32_16x16x32_bf16 a[196:199], a[12:15], a[112:115], a[196:199]\n"
    "ds_read_b64_tr_b16 a[60:61], v11 offset:8232\n"
    "v_mfma_f32_16x16x32_bf16 a[196:199], a[28:31], a[116:119], a[196:199]\n"
    "ds_read_b64_tr_b16 a[62:63], v11 offset:9256\n"
    "s_cbranch_scc1 CBS_LOOP\n"
    "s_waitcnt lgkmcnt(10)\n"
    "v_mfma_f32_16x16x32_bf16 v[64:67], a[32:35], a[96:99], v[64:67]\n"
    "ds_read_b64_tr_b16 a[76:77], v9 offset:8\n"
    "ds_read_b64_tr_b16 a[78:79], v9 offset:1032\n"
    "v_mfma_f32_16x16x32_bf16 v[64:67], a[48:51], a[100:103], v[64:67]\n"
    "ds_read_b64_tr_b16 a[92:93], v9 offset:8232\n"
    "v_mfma_f32_16x16x32_bf16 v[64:67], a[64:67], a[104:107], v[64:67]\n"
    "ds_read_b64_tr_b16 a[94:95], v9 offset:9256\n"
    "v_mfma_f32_16x16x32_bf16 v[64:67], a[80:83], a[108:111], v[64:67]\n"
    "ds_read_b128 a[112:115], v2 offset:256\n"
    "ds_read_b128 a[116:119], v2 offset:320\n"
    "v_mfma_f32_16x16x32_bf16 v[68:71], a[36:39], a[96:99], v[68:71]\n"
    "ds_read_b64_tr_b16 a[0:1], v6 offset:0\n"
    "v_mfma_f32_16x16x32_bf16 v[68:71], a[52:55], a[100:103], v[68:71]\n"
    "ds_read_b64_tr_b16 a[2:3], v6 offset:1024\n"
    "v_mfma_f32_16x16x32_bf16 v[68:71], a[68:71], a[104:107], v[68:71]\n"
    "ds_read_b64_tr_b16 a[16:17], v6 offset:8224\n"
    "ds_read_b64_tr_b16 a[18:19], v6 offset:9248\n"
    "v_mfma_f32_16x16x32_bf16 v[68:71], a[84:87], a[108:111], v[68:71]\n"
    "ds_read_b64_tr_b16 a[4:5], v6 offset:8\n"
    "v_mfma_f32_16x16x32_bf16 v[72:75], a[40:43], a[96:99], v[72:75]\n"
    "ds_read_b64_tr_b16 a[6:7], v6 offset:1032\n"
    "s_waitcnt lgkmcnt(10)\n"
    "v_mfma_f32_16x16x32_bf16 v[72:75], a[56:59], a[100:103], v[72:75]\n"
    "ds_read_b64_tr_b16 a[20:21], v6 offset:8232\n"
    "v_pk_fma_f32 v[64:65], v[64:65], v[32:33], v[48:49]\n"
    "v_pk_fma_f32 v[66:67], v[66:67], v[34:35], v[50:51]\n"
    "s_nop 3\n"
    "v_pk_fma_f32 v[68:69], v[68:69], v[36:37], v[52:53]\n"
    "v_pk_fma_f32 v[70:71], v[70:71], v[38:39], v[54:55]\n"
    "v_exp_f32 v24, v64\n"
    "v_mfma_f32_16x16x32_bf16 v[72:75], a[72:75], a[104:107], v[72:75]\n"
    "ds_read_b64_tr_b16 a[22:23], v6 offset:9256\n"
    "v_exp_f32 v25, v65\n"
    "v_mfma_f32_16x16x32_bf16 v[72:75], a[88:91], a[108:111], v[72:75]\n"
    "ds_read_b64_tr_b16 a[8:9], v7 offset:0\n"
    "v_exp_f32 v26, v66\n"
    "v_mfma_f32_16x16x32_bf16 v[76:79], a[44:47], a[96:99], v[76:79]\n"
    "ds_read_b64_tr_b16 a[10:11], v7 offset:1024\n"
    "v_exp_f32 v27, v67\n"
    "v_mfma_f32_16x16x32_bf16 v[76:79], a[60:63], a[100:103], v[76:79]\n"
    "ds_read_b64_tr_b16 a[24:25], v7 offset:8224\n"
    "v_exp_f32 v28, v68\n"
    "v_mfma_f32_16x16x32_bf16 v[76:79], a[76:79], a[104:107], v[76:79]\n"
    "ds_read_b64_tr_b16 a[26:27], v7 offset:9248\n"
    "v_exp_f32 v29, v69\n"
    "s_waitcnt lgkmcnt(8)\n"
    "v_mfma_f32_16x16x32_bf16 v[76:79], a[92:95], a[108:111], v[76:79]\n"
    "ds_read_b64_tr_b16 a[12:13], v7 offset:8\n"
    "v_mfma_f32_16x16x32_bf16 v[80:83], a[32:35], a[104:107], v[80:83]\n"
    "ds_read_b64_tr_b16 a[14:15], v7 offset:1032\n"
    "v_mfma_f32_16x16x32_bf16 v[80:83], a[48:51], a[108:111], v[80:83]\n"
    "ds_read_b64_tr_b16 a[28:29], v7 offset:8232\n"
    "v_mfma_f32_16x16x32_bf16 v[80:83], a[64:67], a[112:115], v[80:83]\n"
    "ds_read_b64_tr_b16 a[30:31], v7 offset:9256\n"
    "v_mfma_f32_16x16x32_bf16 v[80:83], a[80:83], a[116:119], v[80:83]\n"
    "ds_read_b128 a[120:123], v2 offset:384\n"
    "v_mfma_f32_16x16x32_bf16 v[80:83], a[0:3], a[96:99], v[80:83]\n"
    "ds_read_b128 a[124:127], v2 offset:448\n"
    "v_mfma_f32_16x16x32_bf16 v[80:83], a[16:19], a[100:103], v[80:83]\n"
    "v_exp_f32 v30, v70\n"
    "v_exp_f32 v31, v71\n"
    "v_pk_fma_f32 v[72:73], v[72:73], v[40:41], v[56:57]\n"
    "v_pk_fma_f32 v[74:75], v[74:75], v[42:43], v[58:59]\n"
    "v_pk_fma_f32 v[76:77], v[76:77], v[44:45], v[60:61]\n"
    "v_pk_fma_f32 v[78:79], v[78:79], v[46:47], v[62:63]\n"
    "v_pk_fma_f32 v[24:25], v[24:25], s[54:55], s[54:55]\n"
    "v_pk_fma_f32 v[26:27], v[26:27], s[54:55], s[54:55]\n"
    "v_pk_fma_f32 v[28:29], v[28:29], s[54:55], s[54:55]\n"
    "v_pk_fma_f32 v[30:31], v[30:31], s[54:55], s[54:55]\n"
    "v_exp_f32 v16, v72\n"
    "v_mfma_f32_16x16x32_bf16 v[84:87], a[36:39], a[104:107], v[84:87]\n"
    "v_exp_f32 v17, v73\n"
    "v_exp_f32 v18, v74\n"
    "v_mfma_f32_16x16x32_bf16 v[84:87], a[52:55], a[108:111], v[84:87]\n"
    "v_exp_f32 v19, v75\n"
    "v_mfma_f32_16x16x32_bf16 v[84:87], a[68:71], a[112:115], v[84:87]\n"
    "v_exp_f32 v20, v76\n"
    "v_mfma_f32_16x16x32_bf16 v[84:87], a[84:87], a[116:119], v[84:87]\n"
    "v_exp_f32 v21, v77\n"
    "s_waitcnt lgkmcnt(3)\n"
    "v_mfma_f32_16x16x32_bf16 v[84:87], a[4:7], a[96:99], v[84:87]\n"
    "v_exp_f32 v22, v78\n"
    "v_mfma_f32_16x16x32_bf16 v[84:87], a[20:23], a[100:103], v[84:87]\n"
    "v_exp_f32 v23, v79\n"
    "v_rcp_f32 v24, v24\n"
    "v_mfma_f32_16x16x32_bf16 v[88:91], a[40:43], a[104:107], v[88:91]\n"
    "v_rcp_f32 v25, v25\n"
    "v_mfma_f32_16x16x32_bf16 v[88:91], a[56:59], a[108:111], v[88:91]\n"
    "v_rcp_f32 v26, v26\n"
    "v_mfma_f32_16x16x32_bf16 v[88:91], a[72:75], a[112:115], v[88:91]\n"
    "v_rcp_f32 v27, v27\n"
    "v_mfma_f32_16x16x32_bf16 v[88:91], a[88:91], a[116:119], v[88:91]\n"
    "v_rcp_f32 v28, v28\n"
    "v_mfma_f32_16x16x32_bf16 v[88:91], a[8:11], a[96:99], v[88:91]\n"
    "v_rcp_f32 v29, v29\n"
    "v_mfma_f32_16x16x32_bf16 v[88:91], a[24:27], a[100:103], v[88:91]\n"
    "v_rcp_f32 v30, v30\n"
    "v_rcp_f32 v31, v31\n"
    "v_pk_fma_f32 v[80:81], v[80:81], v[32:33], v[48:49]\n"
    "v_pk_fma_f32 v[82:83], v[82:83], v[34:35], v[50:51]\n"
    "v_pk_fma_f32 v[84:85], v[84:85], v[36:37], v[52:53]\n"
    "v_pk_fma_f32 v[86:87], v[86:87], v[38:39], v[54:55]\n"
    "v_pk_mul_f32 v[64:65], v[64:65], v[24:25]\n"
    "v_pk_mul_f32 v[66:67], v[66:67], v[26:27]\n"
    "v_pk_mul_f32 v[68:69], v[68:69], v[28:29]\n"
    "v_pk_mul_f32 v[70:71], v[70:71], v[30:31]\n"
    "v_pk_fma_f32 v[16:17], v[16:17], s[54:55], s[54:55]\n"
    "v_pk_fma_f32 v[18:19], v[18:19], s[54:55], s[54:55]\n"
    "v_pk_fma_f32 v[20:21], v[20:21], s[54:55], s[54:55]\n"
    "v_pk_fma_f32 v[22:23], v[22:23], s[54:55], s[54:55]\n"
    "v_cvt_pk_bf16_f32 v64, v64, v65\n"
    "v_cvt_pk_bf16_f32 v65, v66, v67\n"
    "v_exp_f32 v8, v80\n"
    "v_mfma_f32_16x16x32_bf16 v[92:95], a[44:47], a[104:107], v[92:95]\n"
    "v_cvt_pk_bf16_f32 v66, v68, v69\n"
    "v_cvt_pk_bf16_f32 v67, v70, v71\n"
    "buffer_store_dwordx4 v[64:67], v1, s[64:67], s51 offen offset:0 sc1\n"
    "v_exp_f32 v9, v81\n"
    "v_mfma_f32_16x16x32_bf16 v[92:95], a[60:63], a[108:111], v[92:95]\n"
    "v_exp_f32 v10, v82\n"
    "v_mfma_f32_16x16x32_bf16 v[92:95], a[76:79], a[112:115], v[92:95]\n"
    "v_exp_f32 v11, v83\n"
    "v_mfma_f32_16x16x32_bf16 v[92:95], a[92:95], a[116:119], v[92:95]\n"
    "v_exp_f32 v12, v84\n"
    "v_exp_f32 v13, v85\n"
    "v_mfma_f32_16x16x32_bf16 v[92:95], a[12:15], a[96:99], v[92:95]\n"
    "v_exp_f32 v14, v86\n"
    "s_waitcnt lgkmcnt(0)\n"
    "v_mfma_f32_16x16x32_bf16 v[92:95], a[28:31], a[100:103], v[92:95]\n"
    "v_exp_f32 v15, v87\n"
    "v_mfma_f32_16x16x32_bf16 v[96:99], a[32:35], a[112:115], v[96:99]\n"
    "v_rcp_f32 v16, v16\n"
    "v_mfma_f32_16x16x32_bf16 v[96:99], a[48:51], a[116:119], v[96:99]\n"
    "v_rcp_f32 v17, v17\n"
    "v_rcp_f32 v18, v18\n"
    "v_mfma_f32_16x16x32_bf16 v[96:99], a[64:67], a[120:123], v[96:99]\n"
    "v_rcp_f32 v19, v19\n"
    "v_mfma_f32_16x16x32_bf16 v[96:99], a[80:83], a[124:127], v[96:99]\n"
    "ds_read_b128 a[96:99], v2 offset:512\n"
    "v_rcp_f32 v20, v20\n"
    "v_mfma_f32_16x16x32_bf16 v[96:99], a[0:3], a[104:107], v[96:99]\n"
    "v_rcp_f32 v21, v21\n"
    "v_mfma_f32_16x16x32_bf16 v[96:99], a[16:19], a[108:111], v[96:99]\n"
    "ds_read_b128 a[100:103], v2 offset:576\n"
    "v_rcp_f32 v22, v22\n"
    "v_rcp_f32 v23, v23\n"
    "v_pk_fma_f32 v[88:89], v[88:89], v[40:41], v[56:57]\n"
    "v_pk_fma_f32 v[90:91], v[90:91], v[42:43], v[58:59]\n"
    "v_pk_fma_f32 v[92:93], v[92:93], v[44:45], v[60:61]\n"
    "v_pk_fma_f32 v[94:95], v[94:95], v[46:47], v[62:63]\n"
    "v_pk_mul_f32 v[72:73], v[72:73], v[16:17]\n"
    "v_pk_mul_f32 v[74:75], v[74:75], v[18:19]\n"
    "v_pk_mul_f32 v[76:77], v[76:77], v[20:21]\n"
    "v_pk_mul_f32 v[78:79], v[78:79], v[22:23]\n"
    "v_pk_fma_f32 v[8:9], v[8:9], s[54:55], s[54:55]\n"
    "v_pk_fma_f32 v[10:11], v[10:11], s[54:55], s[54:55]\n"
    "v_pk_fma_f32 v[12:13], v[12:13], s[54:55], s[54:55]\n"
    "v_pk_fma_f32 v[14:15], v[14:15], s[54:55], s[54:55]\n"
    "v_cvt_pk_bf16_f32 v72, v72, v73\n"
    "v_cvt_pk_bf16_f32 v73, v74, v75\n"
    "v_exp_f32 v24, v88\n"
    "v_mfma_f32_16x16x32_bf16 v[100:103], a[36:39], a[112:115], v[100:103]\n"
    "v_cvt_pk_bf16_f32 v74, v76, v77\n"
    "v_cvt_pk_bf16_f32 v75, v78, v79\n"
    "buffer_store_dwordx4 v[72:75], v1, s[64:67], s51 offen offset:64 sc1\n"
    "s_add_u32 s51, s51, s52\n"
    "v_exp_f32 v25, v89\n"
    "v_mfma_f32_16x16x32_bf16 v[100:103], a[52:55], a[116:119], v[100:103]\n"
    "v_exp_f32 v26, v90\n"
    "v_mfma_f32_16x16x32_bf16 v[100:103], a[68:71], a[120:123], v[100:103]\n"
    "v_exp_f32 v27, v91\n"
    "v_mfma_f32_16x16x32_bf16 v[100:103], a[84:87], a[124:127], v[100:103]\n"
    "v_exp_f32 v28, v92\n"
    "v_exp_f32 v29, v93\n"
    "v_mfma_f32_16x16x32_bf16 v[100:103], a[4:7], a[104:107], v[100:103]\n"
    "v_exp_f32 v30, v94\n"
    "v_mfma_f32_16x16x32_bf16 v[100:103], a[20:23], a[108:111], v[100:103]\n"
    "v_exp_f32 v31, v95\n"
    "v_mfma_f32_16x16x32_bf16 v[104:107], a[40:43], a[112:115], v[104:107]\n"
    "v_rcp_f32 v8, v8\n"
    "v_mfma_f32_16x16x32_bf16 v[104:107], a[56:59], a[116:119], v[104:107]\n"
    "v_rcp_f32 v9, v9\n"
    "v_rcp_f32 v10, v10\n"
    "v_mfma_f32_16x16x32_bf16 v[104:107], a[72:75], a[120:123], v[104:107]\n"
    "v_rcp_f32 v11, v11\n"
    "v_mfma_f32_16x16x32_bf16 v[104:107], a[88:91], a[124:127], v[104:107]\n"
    "v_rcp_f32 v12, v12\n"
    "v_mfma_f32_16x16x32_bf16 v[104:107], a[8:11], a[104:107], v[104:107]\n"
    "v_rcp_f32 v13, v13\n"
    "v_mfma_f32_16x16x32_bf16 v[104:107], a[24:27], a[108:111], v[104:107]\n"
    "v_rcp_f32 v14, v14\n"
    "v_rcp_f32 v15, v15\n"
    "v_pk_fma_f32 v[96:97], v[96:97], v[32:33], v[48:49]\n"
    "v_pk_fma_f32 v[98:99], v[98:99], v[34:35], v[50:51]\n"
    "v_pk_fma_f32 v[100:101], v[100:101], v[36:37], v[52:53]\n"
    "v_pk_fma_f32 v[102:103], v[102:103], v[38:39], v[54:55]\n"
    "v_pk_mul_f32 v[80:81], v[80:81], v[8:9]\n"
    "v_pk_mul_f32 v[82:83], v[82:83], v[10:11]\n"
    "v_pk_mul_f32 v[84:85], v[84:85], v[12:13]\n"
    "v_pk_mul_f32 v[86:87], v[86:87], v[14:15]\n"
    "v_pk_fma_f32 v[24:25], v[24:25], s[54:55], s[54:55]\n"
    "v_pk_fma_f32 v[26:27], v[26:27], s[54:55], s[54:55]\n"
    "v_pk_fma_f32 v[28:29], v[28:29], s[54:55], s[54:55]\n"
    "v_pk_fma_f32 v[30:31], v[30:31], s[54:55], s[54:55]\n"
    "v_cvt_pk_bf16_f32 v80, v80, v81\n"
    "v_cvt_pk_bf16_f32 v81, v82, v83\n"
    "v_exp_f32 v16, v96\n"
    "v_mfma_f32_16x16x32_bf16 v[108:111], a[44:47], a[112:115], v[108:111]\n"
    "v_cvt_pk_bf16_f32 v82, v84, v85\n"
    "v_cvt_pk_bf16_f32 v83, v86, v87\n"
    "buffer_store_dwordx4 v[80:83], v1, s[64:67], s51 offen offset:0 sc1\n"
    "v_exp_f32 v17, v97\n"
    "v_mfma_f32_16x16x32_bf16 v[108:111], a[60:63], a[116:119], v[108:111]\n"
    "v_exp_f32 v18, v98\n"
    "v_mfma_f32_16x16x32_bf16 v[108:111], a[76:79], a[120:123], v[108:111]\n"
    "v_exp_f32 v19, v99\n"
    "v_mfma_f32_16x16x32_bf16 v[108:111], a[92:95], a[124:127], v[108:111]\n"
    "v_exp_f32 v20, v100\n"
    "v_exp_f32 v21, v101\n"
    "v_mfma_f32_16x16x32_bf16 v[108:111], a[12:15], a[104:107], v[108:111]\n"
    "v_exp_f32 v22, v102\n"
    "v_mfma_f32_16x16x32_bf16 v[108:111], a[28:31], a[108:111], v[108:111]\n"
    "v_exp_f32 v23, v103\n"
    "v_mfma_f32_16x16x32_bf16 v[112:115], a[32:35], a[120:123], v[112:115]\n"
    "v_rcp_f32 v24, v24\n"
    "v_mfma_f32_16x16x32_bf16 v[112:115], a[48:51], a[124:127], v[112:115]\n"
    "ds_read_b128 a[104:107], v2 offset:640\n"
    "v_rcp_f32 v25, v25\n"
    "v_rcp_f32 v26, v26\n"
    "s_waitcnt lgkmcnt(1)\n"
    "v_mfma_f32_16x16x32_bf16 v[112:115], a[64:67], a[96:99], v[112:115]\n"
    "v_rcp_f32 v27, v27\n"
    "v_mfma_f32_16x16x32_bf16 v[112:115], a[80:83], a[100:103], v[112:115]\n"
    "ds_read_b128 a[108:111], v2 offset:704\n"
    "v_rcp_f32 v28, v28\n"
    "v_mfma_f32_16x16x32_bf16 v[112:115], a[0:3], a[112:115], v[112:115]\n"
    "v_rcp_f32 v29, v29\n"
    "v_mfma_f32_16x16x32_bf16 v[112:115], a[16:19], a[116:119], v[112:115]\n"
    "v_rcp_f32 v30, v30\n"
    "v_rcp_f32 v31, v31\n"
    "v_pk_fma_f32 v[104:105], v[104:105], v[40:41], v[56:57]\n"
    "v_pk_fma_f32 v[106:107], v[106:107], v[42:43], v[58:59]\n"
    "v_pk_fma_f32 v[108:109], v[108:109], v[44:45], v[60:61]\n"
    "v_pk_fma_f32 v[110:111], v[110:111], v[46:47], v[62:63]\n"
    "v_pk_mul_f32 v[88:89], v[88:89], v[24:25]\n"
    "v_pk_mul_f32 v[90:91], v[90:91], v[26:27]\n"
    "v_pk_mul_f32 v[92:93], v[92:93], v[28:29]\n"
    "v_pk_mul_f32 v[94:95], v[94:95], v[30:31]\n"
    "v_pk_fma_f32 v[16:17], v[16:17], s[54:55], s[54:55]\n"
    "v_pk_fma_f32 v[18:19], v[18:19], s[54:55], s[54:55]\n"
    "v_pk_fma_f32 v[20:21], v[20:21], s[54:55], s[54:55]\n"
    "v_pk_fma_f32 v[22:23], v[22:23], s[54:55], s[54:55]\n"
    "v_cvt_pk_bf16_f32 v88, v88, v89\n"
    "v_cvt_pk_bf16_f32 v89, v90, v91\n"
    "v_exp_f32 v8, v104\n"
    "v_mfma_f32_16x16x32_bf16 v[116:119], a[36:39], a[120:123], v[116:119]\n"
    "v_cvt_pk_bf16_f32 v90, v92, v93\n"
    "v_cvt_pk_bf16_f32 v91, v94, v95\n"
    "buffer_store_dwordx4 v[88:91], v1, s[64:67], s51 offen offset:64 sc1\n"
    "s_add_u32 s51, s51, s52\n"
    "v_exp_f32 v9, v105\n"
    "v_mfma_f32_16x16x32_bf16 v[116:119], a[52:55], a[124:127], v[116:119]\n"
    "v_exp_f32 v10, v106\n"
    "v_mfma_f32_16x16x32_bf16 v[116:119], a[68:71], a[96:99], v[116:119]\n"
    "v_exp_f32 v11, v107\n"
    "v_mfma_f32_16x16x32_bf16 v[116:119], a[84:87], a[100:103], v[116:119]\n"
    "v_exp_f32 v12, v108\n"
    "v_exp_f32 v13, v109\n"
    "v_mfma_f32_16x16x32_bf16 v[116:119], a[4:7], a[112:115], v[116:119]\n"
    "v_exp_f32 v14, v110\n"
    "v_mfma_f32_16x16x32_bf16 v[116:119], a[20:23], a[116:119], v[116:119]\n"
    "v_exp_f32 v15, v111\n"
    "v_mfma_f32_16x16x32_bf16 v[120:123], a[40:43], a[120:123], v[120:123]\n"
    "v_rcp_f32 v16, v16\n"
    "v_mfma_f32_16x16x32_bf16 v[120:123], a[56:59], a[124:127], v[120:123]\n"
    "v_rcp_f32 v17, v17\n"
    "v_rcp_f32 v18, v18\n"
    "v_mfma_f32_16x16x32_bf16 v[120:123], a[72:75], a[96:99], v[120:123]\n"
    "v_rcp_f32 v19, v19\n"
    "v_mfma_f32_16x16x32_bf16 v[120:123], a[88:91], a[100:103], v[120:123]\n"
    "v_rcp_f32 v20, v20\n"
    "v_mfma_f32_16x16x32_bf16 v[120:123], a[8:11], a[112:115], v[120:123]\n"
    "v_rcp_f32 v21, v21\n"
    "v_mfma_f32_16x16x32_bf16 v[120:123], a[24:27], a[116:119], v[120:123]\n"
    "v_rcp_f32 v22, v22\n"
    "v_rcp_f32 v23, v23\n"
    "v_pk_fma_f32 v[112:113], v[112:113], v[32:33], v[48:49]\n"
    "v_pk_fma_f32 v[114:115], v[114:115], v[34:35], v[50:51]\n"
    "v_pk_fma_f32 v[116:117], v[116:117], v[36:37], v[52:53]\n"
    "v_pk_fma_f32 v[118:119], v[118:119], v[38:39], v[54:55]\n"
    "v_pk_mul_f32 v[96:97], v[96:97], v[16:17]\n"
    "v_pk_mul_f32 v[98:99], v[98:99], v[18:19]\n"
    "v_pk_mul_f32 v[100:101], v[100:101], v[20:21]\n"
    "v_pk_mul_f32 v[102:103], v[102:103], v[22:23]\n"
    "v_pk_fma_f32 v[8:9], v[8:9], s[54:55], s[54:55]\n"
    "v_pk_fma_f32 v[10:11], v[10:11], s[54:55], s[54:55]\n"
    "v_pk_fma_f32 v[12:13], v[12:13], s[54:55], s[54:55]\n"
    "v_pk_fma_f32 v[14:15], v[14:15], s[54:55], s[54:55]\n"
    "v_cvt_pk_bf16_f32 v96, v96, v97\n"
    "v_cvt_pk_bf16_f32 v97, v98, v99\n"
    "v_exp_f32 v24, v112\n"
    "v_mfma_f32_16x16x32_bf16 v[124:127], a[44:47], a[120:123], v[124:127]\n"
    "v_cvt_pk_bf16_f32 v98, v100, v101\n"
    "v_cvt_pk_bf16_f32 v99, v102, v103\n"
    "buffer_store_dwordx4 v[96:99], v1, s[64:67], s51 offen offset:0 sc1\n"
    "v_exp_f32 v25, v113\n"
    "v_mfma_f32_16x16x32_bf16 v[124:127], a[60:63], a[124:127], v[124:127]\n"
    "v_exp_f32 v26, v114\n"
    "v_mfma_f32_16x16x32_bf16 v[124:127], a[76:79], a[96:99], v[124:127]\n"
    "v_exp_f32 v27, v115\n"
    "v_mfma_f32_16x16x32_bf16 v[124:127], a[92:95], a[100:103], v[124:127]\n"
    "v_exp_f32 v28, v116\n"
    "v_exp_f32 v29, v117\n"
    "v_mfma_f32_16x16x32_bf16 v[124:127], a[12:15], a[112:115], v[124:127]\n"
    "v_exp_f32 v30, v118\n"
    "v_mfma_f32_16x16x32_bf16 v[124:127], a[28:31], a[116:119], v[124:127]\n"
    "v_exp_f32 v31, v119\n"
    "v_mfma_f32_16x16x32_bf16 v[128:131], a[32:35], a[96:99], v[128:131]\n"
    "v_rcp_f32 v8, v8\n"
    "v_mfma_f32_16x16x32_bf16 v[128:131], a[48:51], a[100:103], v[128:131]\n"
    "ds_read_b128 a[112:115], v2 offset:768\n"
    "v_rcp_f32 v9, v9\n"
    "v_rcp_f32 v10, v10\n"
    "s_waitcnt lgkmcnt(1)\n"
    "v_mfma_f32_16x16x32_bf16 v[128:131], a[64:67], a[104:107], v[128:131]\n"
    "v_rcp_f32 v11, v11\n"
    "v_mfma_f32_16x16x32_bf16 v[128:131], a[80:83], a[108:111], v[128:131]\n"
    "ds_read_b128 a[116:119], v2 offset:832\n"
    "v_rcp_f32 v12, v12\n"
    "v_mfma_f32_16x16x32_bf16 v[128:131], a[0:3], a[120:123], v[128:131]\n"
    "v_rcp_f32 v13, v13\n"
    "v_mfma_f32_16x16x32_bf16 v[128:131], a[16:19], a[124:127], v[128:131]\n"
    "v_rcp_f32 v14, v14\n"
    "v_rcp_f32 v15, v15\n"
    "v_pk_fma_f32 v[120:121], v[120:121], v[40:41], v[56:57]\n"
    "v_pk_fma_f32 v[122:123], v[122:123], v[42:43], v[58:59]\n"
    "v_pk_fma_f32 v[124:125], v[124:125], v[44:45], v[60:61]\n"
    "v_pk_fma_f32 v[126:127], v[126:127], v[46:47], v[62:63]\n"
    "v_pk_mul_f32 v[104:105], v[104:105], v[8:9]\n"
    "v_pk_mul_f32 v[106:107], v[106:107], v[10:11]\n"
    "v_pk_mul_f32 v[108:109], v[108:109], v[12:13]\n"
    "v_pk_mul_f32 v[110:111], v[110:111], v[14:15]\n"
    "v_pk_fma_f32 v[24:25], v[24:25], s[54:55], s[54:55]\n"
    "v_pk_fma_f32 v[26:27], v[26:27], s[54:55], s[54:55]\n"
    "v_pk_fma_f32 v[28:29], v[28:29], s[54:55], s[54:55]\n"
    "v_pk_fma_f32 v[30:31], v[30:31], s[54:55], s[54:55]\n"
    "v_cvt_pk_bf16_f32 v104, v104, v105\n"
    "v_cvt_pk_bf16_f32 v105, v106, v107\n"
    "v_exp_f32 v16, v120\n"
    "v_mfma_f32_16x16x32_bf16 v[132:135], a[36:39], a[96:99], v[132:135]\n"
    "v_cvt_pk_bf16_f32 v106, v108, v109\n"
    "v_cvt_pk_bf16_f32 v107, v110, v111\n"
    "buffer_store_dwordx4 v[104:107], v1, s[64:67], s51 offen offset:64 sc1\n"
    "s_add_u32 s51, s51, s52\n"
    "v_exp_f32 v17, v121\n"
    "v_mfma_f32_16x16x32_bf16 v[132:135], a[52:55], a[100:103], v[132:135]\n"
    "v_exp_f32 v18, v122\n"
    "v_mfma_f32_16x16x32_bf16 v[132:135], a[68:71], a[104:107], v[132:135]\n"
    "v_exp_f32 v19, v123\n"
    "v_mfma_f32_16x16x32_bf16 v[132:135], a[84:87], a[108:111], v[132:135]\n"
    "v_exp_f32 v20, v124\n"
    "v_exp_f32 v21, v125\n"
    "v_mfma_f32_16x16x32_bf16 v[132:135], a[4:7], a[120:123], v[132:135]\n"
    "v_exp_f32 v22, v126\n"
    "v_mfma_f32_16x16x32_bf16 v[132:135], a[20:23], a[124:127], v[132:135]\n"
    "v_exp_f32 v23, v127\n"
    "v_mfma_f32_16x16x32_bf16 v[136:139], a[40:43], a[96:99], v[136:139]\n"
    "v_rcp_f32 v24, v24\n"
    "v_mfma_f32_16x16x32_bf16 v[136:139], a[56:59], a[100:103], v[136:139]\n"
    "v_rcp_f32 v25, v25\n"
    "v_rcp_f32 v26, v26\n"
    "v_mfma_f32_16x16x32_bf16 v[136:139], a[72:75], a[104:107], v[136:139]\n"
    "v_rcp_f32 v27, v27\n"
    "v_mfma_f32_16x16x32_bf16 v[136:139], a[88:91], a[108:111], v[136:139]\n"
    "v_rcp_f32 v28, v28\n"
    "v_mfma_f32_16x16x32_bf16 v[136:139], a[8:11], a[120:123], v[136:139]\n"
    "v_rcp_f32 v29, v29\n"
    "v_mfma_f32_16x16x32_bf16 v[136:139], a[24:27], a[124:127], v[136:139]\n"
    "v_rcp_f32 v30, v30\n"
    "v_rcp_f32 v31, v31\n"
    "v_pk_fma_f32 v[128:129], v[128:129], v[32:33], v[48:49]\n"
    "v_pk_fma_f32 v[130:131], v[130:131], v[34:35], v[50:51]\n"
    "v_pk_fma_f32 v[132:133], v[132:133], v[36:37], v[52:53]\n"
    "v_pk_fma_f32 v[134:135], v[134:135], v[38:39], v[54:55]\n"
    "v_pk_mul_f32 v[112:113], v[112:113], v[24:25]\n"
    "v_pk_mul_f32 v[114:115], v[114:115], v[26:27]\n"
    "v_pk_mul_f32 v[116:117], v[116:117], v[28:29]\n"
    "v_pk_mul_f32 v[118:119], v[118:119], v[30:31]\n"
    "v_pk_fma_f32 v[16:17], v[16:17], s[54:55], s[54:55]\n"
    "v_pk_fma_f32 v[18:19], v[18:19], s[54:55], s[54:55]\n"
    "v_pk_fma_f32 v[20:21], v[20:21], s[54:55], s[54:55]\n"
    "v_pk_fma_f32 v[22:23], v[22:23], s[54:55], s[54:55]\n"
    "v_cvt_pk_bf16_f32 v112, v112, v113\n"
    "v_cvt_pk_bf16_f32 v113, v114, v115\n"
    "v_exp_f32 v8, v128\n"
    "v_mfma_f32_16x16x32_bf16 v[140:143], a[44:47], a[96:99], v[140:143]\n"
    "v_cvt_pk_bf16_f32 v114, v116, v117\n"
    "v_cvt_pk_bf16_f32 v115, v118, v119\n"
    "buffer_store_dwordx4 v[112:115], v1, s[64:67], s51 offen offset:0 sc1\n"
    "v_exp_f32 v9, v129\n"
    "v_mfma_f32_16x16x32_bf16 v[140:143], a[60:63], a[100:103], v[140:143]\n"
    "v_exp_f32 v10, v130\n"
    "v_mfma_f32_16x16x32_bf16 v[140:143], a[76:79], a[104:107], v[140:143]\n"
    "v_exp_f32 v11, v131\n"
    "v_mfma_f32_16x16x32_bf16 v[140:143], a[92:95], a[108:111], v[140:143]\n"
    "v_exp_f32 v12, v132\n"
    "v_exp_f32 v13, v133\n"
    "v_mfma_f32_16x16x32_bf16 v[140:143], a[12:15], a[120:123], v[140:143]\n"
    "v_exp_f32 v14, v134\n"
    "v_mfma_f32_16x16x32_bf16 v[140:143], a[28:31], a[124:127], v[140:143]\n"
    "v_exp_f32 v15, v135\n"
    "v_mfma_f32_16x16x32_bf16 v[144:147], a[32:35], a[104:107], v[144:147]\n"
    "v_rcp_f32 v16, v16\n"
    "v_mfma_f32_16x16x32_bf16 v[144:147], a[48:51], a[108:111], v[144:147]\n"
    "ds_read_b128 a[120:123], v2 offset:896\n"
    "v_rcp_f32 v17, v17\n"
    "v_rcp_f32 v18, v18\n"
    "s_waitcnt lgkmcnt(1)\n"
    "v_mfma_f32_16x16x32_bf16 v[144:147], a[64:67], a[112:115], v[144:147]\n"
    "v_rcp_f32 v19, v19\n"
    "v_mfma_f32_16x16x32_bf16 v[144:147], a[80:83], a[116:119], v[144:147]\n"
    "ds_read_b128 a[124:127], v2 offset:960\n"
    "v_rcp_f32 v20, v20\n"
    "v_mfma_f32_16x16x32_bf16 v[144:147], a[0:3], a[96:99], v[144:147]\n"
    "v_rcp_f32 v21, v21\n"
    "v_mfma_f32_16x16x32_bf16 v[144:147], a[16:19], a[100:103], v[144:147]\n"
    "v_rcp_f32 v22, v22\n"
    "v_rcp_f32 v23, v23\n"
    "v_pk_fma_f32 v[136:137], v[136:137], v[40:41], v[56:57]\n"
    "v_pk_fma_f32 v[138:139], v[138:139], v[42:43], v[58:59]\n"
    "v_pk_fma_f32 v[140:141], v[140:141], v[44:45], v[60:61]\n"
    "v_pk_fma_f32 v[142:143], v[142:143], v[46:47], v[62:63]\n"
    "v_pk_mul_f32 v[120:121], v[120:121], v[16:17]\n"
    "v_pk_mul_f32 v[122:123], v[122:123], v[18:19]\n"
    "v_pk_mul_f32 v[124:125], v[124:125], v[20:21]\n"
    "v_pk_mul_f32 v[126:127], v[126:127], v[22:23]\n"
    "v_pk_fma_f32 v[8:9], v[8:9], s[54:55], s[54:55]\n"
    "v_pk_fma_f32 v[10:11], v[10:11], s[54:55], s[54:55]\n"
    "v_pk_fma_f32 v[12:13], v[12:13], s[54:55], s[54:55]\n"
    "v_pk_fma_f32 v[14:15], v[14:15], s[54:55], s[54:55]\n"
    "v_cvt_pk_bf16_f32 v120, v120, v121\n"
    "v_cvt_pk_bf16_f32 v121, v122, v123\n"
    "v_exp_f32 v24, v136\n"
    "v_mfma_f32_16x16x32_bf16 v[148:151], a[36:39], a[104:107], v[148:151]\n"
    "v_cvt_pk_bf16_f32 v122, v124, v125\n"
    "v_cvt_pk_bf16_f32 v123, v126, v127\n"
    "buffer_store_dwordx4 v[120:123], v1, s[64:67], s51 offen offset:64 sc1\n"
    "s_add_u32 s51, s51, s52\n"
    "v_exp_f32 v25, v137\n"
    "v_mfma_f32_16x16x32_bf16 v[148:151], a[52:55], a[108:111], v[148:151]\n"
    "v_exp_f32 v26, v138\n"
    "v_mfma_f32_16x16x32_bf16 v[148:151], a[68:71], a[112:115], v[148:151]\n"
    "v_exp_f32 v27, v139\n"
    "v_mfma_f32_16x16x32_bf16 v[148:151], a[84:87], a[116:119], v[148:151]\n"
    "v_exp_f32 v28, v140\n"
    "v_exp_f32 v29, v141\n"
    "v_mfma_f32_16x16x32_bf16 v[148:151], a[4:7], a[96:99], v[148:151]\n"
    "v_exp_f32 v30, v142\n"
    "v_mfma_f32_16x16x32_bf16 v[148:151], a[20:23], a[100:103], v[148:151]\n"
    "v_exp_f32 v31, v143\n"
    "v_mfma_f32_16x16x32_bf16 v[152:155], a[40:43], a[104:107], v[152:155]\n"
    "v_rcp_f32 v8, v8\n"
    "v_mfma_f32_16x16x32_bf16 v[152:155], a[56:59], a[108:111], v[152:155]\n"
    "v_rcp_f32 v9, v9\n"
    "v_rcp_f32 v10, v10\n"
    "v_mfma_f32_16x16x32_bf16 v[152:155], a[72:75], a[112:115], v[152:155]\n"
    "v_rcp_f32 v11, v11\n"
    "v_mfma_f32_16x16x32_bf16 v[152:155], a[88:91], a[116:119], v[152:155]\n"
    "v_rcp_f32 v12, v12\n"
    "v_mfma_f32_16x16x32_bf16 v[152:155], a[8:11], a[96:99], v[152:155]\n"
    "v_rcp_f32 v13, v13\n"
    "v_mfma_f32_16x16x32_bf16 v[152:155], a[24:27], a[100:103], v[152:155]\n"
    "v_rcp_f32 v14, v14\n"
    "v_rcp_f32 v15, v15\n"
    "v_pk_fma_f32 v[144:145], v[144:145], v[32:33], v[48:49]\n"
    "v_pk_fma_f32 v[146:147], v[146:147], v[34:35], v[50:51]\n"
    "v_pk_fma_f32 v[148:149], v[148:149], v[36:37], v[52:53]\n"
    "v_pk_fma_f32 v[150:151], v[150:151], v[38:39], v[54:55]\n"
    "v_pk_mul_f32 v[128:129], v[128:129], v[8:9]\n"
    "v_pk_mul_f32 v[130:131], v[130:131], v[10:11]\n"
    "v_pk_mul_f32 v[132:133], v[132:133], v[12:13]\n"
    "v_pk_mul_f32 v[134:135], v[134:135], v[14:15]\n"
    "v_pk_fma_f32 v[24:25], v[24:25], s[54:55], s[54:55]\n"
    "v_pk_fma_f32 v[26:27], v[26:27], s[54:55], s[54:55]\n"
    "v_pk_fma_f32 v[28:29], v[28:29], s[54:55], s[54:55]\n"
    "v_pk_fma_f32 v[30:31], v[30:31], s[54:55], s[54:55]\n"
    "v_cvt_pk_bf16_f32 v128, v128, v129\n"
    "v_cvt_pk_bf16_f32 v129, v130, v131\n"
    "v_exp_f32 v16, v144\n"
    "v_mfma_f32_16x16x32_bf16 v[156:159], a[44:47], a[104:107], v[156:159]\n"
    "v_cvt_pk_bf16_f32 v130, v132, v133\n"
    "v_cvt_pk_bf16_f32 v131, v134, v135\n"
    "buffer_store_dwordx4 v[128:131], v1, s[64:67], s51 offen offset:0 sc1\n"
    "v_exp_f32 v17, v145\n"
    "v_mfma_f32_16x16x32_bf16 v[156:159], a[60:63], a[108:111], v[156:159]\n"
    "v_exp_f32 v18, v146\n"
    "v_mfma_f32_16x16x32_bf16 v[156:159], a[76:79], a[112:115], v[156:159]\n"
    "v_exp_f32 v19, v147\n"
    "v_mfma_f32_16x16x32_bf16 v[156:159], a[92:95], a[116:119], v[156:159]\n"
    "v_exp_f32 v20, v148\n"
    "v_exp_f32 v21, v149\n"
    "v_mfma_f32_16x16x32_bf16 v[156:159], a[12:15], a[96:99], v[156:159]\n"
    "v_exp_f32 v22, v150\n"
    "v_mfma_f32_16x16x32_bf16 v[156:159], a[28:31], a[100:103], v[156:159]\n"
    "s_waitcnt vmcnt(9) lgkmcnt(0)\n"
    "s_barrier\n"
    "v_add_u32 v2, s41, v4\n"
    "v_exp_f32 v23, v151\n"
    "v_mfma_f32_16x16x32_bf16 v[160:163], a[32:35], a[112:115], v[160:163]\n"
    "v_rcp_f32 v24, v24\n"
    "v_mfma_f32_16x16x32_bf16 v[160:163], a[48:51], a[116:119], v[160:163]\n"
    "ds_read_b128 a[96:99], v2 offset:0\n"
    "v_rcp_f32 v25, v25\n"
    "v_rcp_f32 v26, v26\n"
    "v_mfma_f32_16x16x32_bf16 v[160:163], a[64:67], a[120:123], v[160:163]\n"
    "v_rcp_f32 v27, v27\n"
    "v_mfma_f32_16x16x32_bf16 v[160:163], a[80:83], a[124:127], v[160:163]\n"
    "ds_read_b128 a[100:103], v2 offset:64\n"
    "v_add_u32 v3, s41, v5\n"
    "v_rcp_f32 v28, v28\n"
    "v_mfma_f32_16x16x32_bf16 v[160:163], a[0:3], a[104:107], v[160:163]\n"
    "v_rcp_f32 v29, v29\n"
    "v_mfma_f32_16x16x32_bf16 v[160:163], a[16:19], a[108:111], v[160:163]\n"
    "v_rcp_f32 v30, v30\n"
    "v_rcp_f32 v31, v31\n"
    "v_pk_fma_f32 v[152:153], v[152:153], v[40:41], v[56:57]\n"
    "v_pk_fma_f32 v[154:155], v[154:155], v[42:43], v[58:59]\n"
    "v_pk_fma_f32 v[156:157], v[156:157], v[44:45], v[60:61]\n"
    "v_pk_fma_f32 v[158:159], v[158:159], v[46:47], v[62:63]\n"
    "v_pk_mul_f32 v[136:137], v[136:137], v[24:25]\n"
    "v_pk_mul_f32 v[138:139], v[138:139], v[26:27]\n"
    "v_pk_mul_f32 v[140:141], v[140:141], v[28:29]\n"
    "v_pk_mul_f32 v[142:143], v[142:143], v[30:31]\n"
    "v_pk_fma_f32 v[16:17], v[16:17], s[54:55], s[54:55]\n"
    "v_pk_fma_f32 v[18:19], v[18:19], s[54:55], s[54:55]\n"
    "v_pk_fma_f32 v[20:21], v[20:21], s[54:55], s[54:55]\n"
    "v_pk_fma_f32 v[22:23], v[22:23], s[54:55], s[54:55]\n"
    "v_cvt_pk_bf16_f32 v136, v136, v137\n"
    "v_cvt_pk_bf16_f32 v137, v138, v139\n"
    "v_exp_f32 v8, v152\n"
    "v_mfma_f32_16x16x32_bf16 v[164:167], a[36:39], a[112:115], v[164:167]\n"
    "v_cvt_pk_bf16_f32 v138, v140, v141\n"
    "v_cvt_pk_bf16_f32 v139, v142, v143\n"
    "buffer_store_dwordx4 v[136:139], v1, s[64:67], s51 offen offset:64 sc1\n"
    "s_add_u32 s51, s51, s52\n"
    "v_exp_f32 v9, v153\n"
    "v_mfma_f32_16x16x32_bf16 v[164:167], a[52:55], a[116:119], v[164:167]\n"
    "v_exp_f32 v10, v154\n"
    "v_mfma_f32_16x16x32_bf16 v[164:167], a[68:71], a[120:123], v[164:167]\n"
    "v_exp_f32 v11, v155\n"
    "v_mfma_f32_16x16x32_bf16 v[164:167], a[84:87], a[124:127], v[164:167]\n"
    "v_exp_f32 v12, v156\n"
    "v_exp_f32 v13, v157\n"
    "v_mfma_f32_16x16x32_bf16 v[164:167], a[4:7], a[104:107], v[164:167]\n"
    "v_exp_f32 v14, v158\n"
    "v_mfma_f32_16x16x32_bf16 v[164:167], a[20:23], a[108:111], v[164:167]\n"
    "v_exp_f32 v15, v159\n"
    "v_mfma_f32_16x16x32_bf16 v[168:171], a[40:43], a[112:115], v[168:171]\n"
    "v_rcp_f32 v16, v16\n"
    "v_mfma_f32_16x16x32_bf16 v[168:171], a[56:59], a[116:119], v[168:171]\n"
    "v_rcp_f32 v17, v17\n"
    "v_rcp_f32 v18, v18\n"
    "v_mfma_f32_16x16x32_bf16 v[168:171], a[72:75], a[120:123], v[168:171]\n"
    "v_rcp_f32 v19, v19\n"
    "v_mfma_f32_16x16x32_bf16 v[168:171], a[88:91], a[124:127], v[168:171]\n"
    "v_rcp_f32 v20, v20\n"
    "v_mfma_f32_16x16x32_bf16 v[168:171], a[8:11], a[104:107], v[168:171]\n"
    "v_rcp_f32 v21, v21\n"
    "v_mfma_f32_16x16x32_bf16 v[168:171], a[24:27], a[108:111], v[168:171]\n"
    "v_rcp_f32 v22, v22\n"
    "v_rcp_f32 v23, v23\n"
    "v_pk_fma_f32 v[160:161], v[160:161], v[32:33], v[48:49]\n"
    "v_pk_fma_f32 v[162:163], v[162:163], v[34:35], v[50:51]\n"
    "v_pk_fma_f32 v[164:165], v[164:165], v[36:37], v[52:53]\n"
    "v_pk_fma_f32 v[166:167], v[166:167], v[38:39], v[54:55]\n"
    "v_pk_mul_f32 v[144:145], v[144:145], v[16:17]\n"
    "v_pk_mul_f32 v[146:147], v[146:147], v[18:19]\n"
    "v_pk_mul_f32 v[148:149], v[148:149], v[20:21]\n"
    "v_pk_mul_f32 v[150:151], v[150:151], v[22:23]\n"
    "v_pk_fma_f32 v[8:9], v[8:9], s[54:55], s[54:55]\n"
    "v_pk_fma_f32 v[10:11], v[10:11], s[54:55], s[54:55]\n"
    "v_pk_fma_f32 v[12:13], v[12:13], s[54:55], s[54:55]\n"
    "v_pk_fma_f32 v[14:15], v[14:15], s[54:55], s[54:55]\n"
    "v_cvt_pk_bf16_f32 v144, v144, v145\n"
    "v_cvt_pk_bf16_f32 v145, v146, v147\n"
    "v_exp_f32 v24, v160\n"
    "v_mfma_f32_16x16x32_bf16 v[172:175], a[44:47], a[112:115], v[172:175]\n"
    "v_cvt_pk_bf16_f32 v146, v148, v149\n"
    "v_cvt_pk_bf16_f32 v147, v150, v151\n"
    "buffer_store_dwordx4 v[144:147], v1, s[64:67], s51 offen offset:0 sc1\n"
    "v_exp_f32 v25, v161\n"
    "v_mfma_f32_16x16x32_bf16 v[172:175], a[60:63], a[116:119], v[172:175]\n"
    "v_exp_f32 v26, v162\n"
    "v_mfma_f32_16x16x32_bf16 v[172:175], a[76:79], a[120:123], v[172:175]\n"
    "v_exp_f32 v27, v163\n"
    "v_mfma_f32_16x16x32_bf16 v[172:175], a[92:95], a[124:127], v[172:175]\n"
    "v_exp_f32 v28, v164\n"
    "v_exp_f32 v29, v165\n"
    "v_mfma_f32_16x16x32_bf16 v[172:175], a[12:15], a[104:107], v[172:175]\n"
    "v_exp_f32 v30, v166\n"
    "v_mfma_f32_16x16x32_bf16 v[172:175], a[28:31], a[108:111], v[172:175]\n"
    "v_exp_f32 v31, v167\n"
    "v_mfma_f32_16x16x32_bf16 v[176:179], a[32:35], a[120:123], v[176:179]\n"
    "v_rcp_f32 v8, v8\n"
    "v_mfma_f32_16x16x32_bf16 v[176:179], a[48:51], a[124:127], v[176:179]\n"
    "ds_read_b128 a[104:107], v2 offset:128\n"
    "v_rcp_f32 v9, v9\n"
    "v_rcp_f32 v10, v10\n"
    "s_waitcnt lgkmcnt(1)\n"
    "v_mfma_f32_16x16x32_bf16 v[176:179], a[64:67], a[96:99], v[176:179]\n"
    "v_rcp_f32 v11, v11\n"
    "v_mfma_f32_16x16x32_bf16 v[176:179], a[80:83], a[100:103], v[176:179]\n"
    "ds_read_b128 a[108:111], v2 offset:192\n"
    "v_rcp_f32 v12, v12\n"
    "v_mfma_f32_16x16x32_bf16 v[176:179], a[0:3], a[112:115], v[176:179]\n"
    "v_rcp_f32 v13, v13\n"
    "v_mfma_f32_16x16x32_bf16 v[176:179], a[16:19], a[116:119], v[176:179]\n"
    "v_rcp_f32 v14, v14\n"
    "v_rcp_f32 v15, v15\n"
    "v_pk_fma_f32 v[168:169], v[168:169], v[40:41], v[56:57]\n"
    "v_pk_fma_f32 v[170:171], v[170:171], v[42:43], v[58:59]\n"
    "v_pk_fma_f32 v[172:173], v[172:173], v[44:45], v[60:61]\n"
    "v_pk_fma_f32 v[174:175], v[174:175], v[46:47], v[62:63]\n"
    "v_pk_mul_f32 v[152:153], v[152:153], v[8:9]\n"
    "v_pk_mul_f32 v[154:155], v[154:155], v[10:11]\n"
    "v_pk_mul_f32 v[156:157], v[156:157], v[12:13]\n"
    "v_pk_mul_f32 v[158:159], v[158:159], v[14:15]\n"
    "v_pk_fma_f32 v[24:25], v[24:25], s[54:55], s[54:55]\n"
    "v_pk_fma_f32 v[26:27], v[26:27], s[54:55], s[54:55]\n"
    "v_pk_fma_f32 v[28:29], v[28:29], s[54:55], s[54:55]\n"
    "v_pk_fma_f32 v[30:31], v[30:31], s[54:55], s[54:55]\n"
    "v_cvt_pk_bf16_f32 v152, v152, v153\n"
    "v_cvt_pk_bf16_f32 v153, v154, v155\n"
    "v_exp_f32 v16, v168\n"
    "v_mfma_f32_16x16x32_bf16 v[180:183], a[36:39], a[120:123], v[180:183]\n"
    "v_cvt_pk_bf16_f32 v154, v156, v157\n"
    "v_cvt_pk_bf16_f32 v155, v158, v159\n"
    "buffer_store_dwordx4 v[152:155], v1, s[64:67], s51 offen offset:64 sc1\n"
    "s_add_u32 s51, s51, s52\n"
    "v_exp_f32 v17, v169\n"
    "v_mfma_f32_16x16x32_bf16 v[180:183], a[52:55], a[124:127], v[180:183]\n"
    "v_exp_f32 v18, v170\n"
    "v_mfma_f32_16x16x32_bf16 v[180:183], a[68:71], a[96:99], v[180:183]\n"
    "v_exp_f32 v19, v171\n"
    "v_mfma_f32_16x16x32_bf16 v[180:183], a[84:87], a[100:103], v[180:183]\n"
    "v_exp_f32 v20, v172\n"
    "v_exp_f32 v21, v173\n"
    "v_mfma_f32_16x16x32_bf16 v[180:183], a[4:7], a[112:115], v[180:183]\n"
    "v_exp_f32 v22, v174\n"
    "v_mfma_f32_16x16x32_bf16 v[180:183], a[20:23], a[116:119], v[180:183]\n"
    "v_exp_f32 v23, v175\n"
    "v_mfma_f32_16x16x32_bf16 v[184:187], a[40:43], a[120:123], v[184:187]\n"
    "v_rcp_f32 v24, v24\n"
    "v_mfma_f32_16x16x32_bf16 v[184:187], a[56:59], a[124:127], v[184:187]\n"
    "v_rcp_f32 v25, v25\n"
    "v_rcp_f32 v26, v26\n"
    "v_mfma_f32_16x16x32_bf16 v[184:187], a[72:75], a[96:99], v[184:187]\n"
    "v_rcp_f32 v27, v27\n"
    "v_mfma_f32_16x16x32_bf16 v[184:187], a[88:91], a[100:103], v[184:187]\n"
    "v_rcp_f32 v28, v28\n"
    "v_mfma_f32_16x16x32_bf16 v[184:187], a[8:11], a[112:115], v[184:187]\n"
    "v_rcp_f32 v29, v29\n"
    "v_mfma_f32_16x16x32_bf16 v[184:187], a[24:27], a[116:119], v[184:187]\n"
    "v_rcp_f32 v30, v30\n"
    "v_rcp_f32 v31, v31\n"
    "v_pk_fma_f32 v[176:177], v[176:177], v[32:33], v[48:49]\n"
    "v_pk_fma_f32 v[178:179], v[178:179], v[34:35], v[50:51]\n"
    "v_pk_fma_f32 v[180:181], v[180:181], v[36:37], v[52:53]\n"
    "v_pk_fma_f32 v[182:183], v[182:183], v[38:39], v[54:55]\n"
    "v_pk_mul_f32 v[160:161], v[160:161], v[24:25]\n"
    "v_pk_mul_f32 v[162:163], v[162:163], v[26:27]\n"
    "v_pk_mul_f32 v[164:165], v[164:165], v[28:29]\n"
    "v_pk_mul_f32 v[166:167], v[166:167], v[30:31]\n"
    "v_pk_fma_f32 v[16:17], v[16:17], s[54:55], s[54:55]\n"
    "v_pk_fma_f32 v[18:19], v[18:19], s[54:55], s[54:55]\n"
    "v_pk_fma_f32 v[20:21], v[20:21], s[54:55], s[54:55]\n"
    "v_pk_fma_f32 v[22:23], v[22:23], s[54:55], s[54:55]\n"
    "v_cvt_pk_bf16_f32 v160, v160, v161\n"
    "v_cvt_pk_bf16_f32 v161, v162, v163\n"
    "v_exp_f32 v8, v176\n"
    "v_mfma_f32_16x16x32_bf16 v[188:191], a[44:47], a[120:123], v[188:191]\n"
    "v_cvt_pk_bf16_f32 v162, v164, v165\n"
    "v_cvt_pk_bf16_f32 v163, v166, v167\n"
    "buffer_store_dwordx4 v[160:163], v1, s[64:67], s51 offen offset:0 sc1\n"
    "v_exp_f32 v9, v177\n"
    "v_mfma_f32_16x16x32_bf16 v[188:191], a[60:63], a[124:127], v[188:191]\n"
    "v_exp_f32 v10, v178\n"
    "v_mfma_f32_16x16x32_bf16 v[188:191], a[76:79], a[96:99], v[188:191]\n"
    "v_exp_f32 v11, v179\n"
    "v_mfma_f32_16x16x32_bf16 v[188:191], a[92:95], a[100:103], v[188:191]\n"
    "v_exp_f32 v12, v180\n"
    "v_exp_f32 v13, v181\n"
    "v_mfma_f32_16x16x32_bf16 v[188:191], a[12:15], a[112:115], v[188:191]\n"
    "v_exp_f32 v14, v182\n"
    "v_mfma_f32_16x16x32_bf16 v[188:191], a[28:31], a[116:119], v[188:191]\n"
    "v_exp_f32 v15, v183\n"
    "v_mfma_f32_16x16x32_bf16 v[192:195], a[32:35], a[96:99], v[192:195]\n"
    "v_rcp_f32 v16, v16\n"
    "v_mfma_f32_16x16x32_bf16 v[192:195], a[48:51], a[100:103], v[192:195]\n"
    "ds_read_b128 a[112:115], v2 offset:256\n"
    "v_rcp_f32 v17, v17\n"
    "v_rcp_f32 v18, v18\n"
    "s_waitcnt lgkmcnt(1)\n"
    "v_mfma_f32_16x16x32_bf16 v[192:195], a[64:67], a[104:107], v[192:195]\n"
    "v_rcp_f32 v19, v19\n"
    "v_mfma_f32_16x16x32_bf16 v[192:195], a[80:83], a[108:111], v[192:195]\n"
    "ds_read_b128 a[116:119], v2 offset:320\n"
    "v_rcp_f32 v20, v20\n"
    "v_mfma_f32_16x16x32_bf16 v[192:195], a[0:3], a[120:123], v[192:195]\n"
    "v_rcp_f32 v21, v21\n"
    "v_mfma_f32_16x16x32_bf16 v[192:195], a[16:19], a[124:127], v[192:195]\n"
    "v_rcp_f32 v22, v22\n"
    "v_rcp_f32 v23, v23\n"
    "v_pk_fma_f32 v[184:185], v[184:185], v[40:41], v[56:57]\n"
    "v_pk_fma_f32 v[186:187], v[186:187], v[42:43], v[58:59]\n"
    "v_pk_fma_f32 v[188:189], v[188:189], v[44:45], v[60:61]\n"
    "v_pk_fma_f32 v[190:191], v[190:191], v[46:47], v[62:63]\n"
    "v_pk_mul_f32 v[168:169], v[168:169], v[16:17]\n"
    "v_pk_mul_f32 v[170:171], v[170:171], v[18:19]\n"
    "v_pk_mul_f32 v[172:173], v[172:173], v[20:21]\n"
    "v_pk_mul_f32 v[174:175], v[174:175], v[22:23]\n"
    "v_pk_fma_f32 v[8:9], v[8:9], s[54:55], s[54:55]\n"
    "v_pk_fma_f32 v[10:11], v[10:11], s[54:55], s[54:55]\n"
    "v_pk_fma_f32 v[12:13], v[12:13], s[54:55], s[54:55]\n"
    "v_pk_fma_f32 v[14:15], v[14:15], s[54:55], s[54:55]\n"
    "v_cvt_pk_bf16_f32 v168, v168, v169\n"
    "v_cvt_pk_bf16_f32 v169, v170, v171\n"
    "v_exp_f32 v24, v184\n"
    "v_mfma_f32_16x16x32_bf16 v[196:199], a[36:39], a[96:99], v[196:199]\n"
    "v_cvt_pk_bf16_f32 v170, v172, v173\n"
    "v_cvt_pk_bf16_f32 v171, v174, v175\n"
    "buffer_store_dwordx4 v[168:171], v1, s[64:67], s51 offen offset:64 sc1\n"
    "s_add_u32 s51, s51, s52\n"
    "v_exp_f32 v25, v185\n"
    "v_mfma_f32_16x16x32_bf16 v[196:199], a[52:55], a[100:103], v[196:199]\n"
    "v_exp_f32 v26, v186\n"
    "v_mfma_f32_16x16x32_bf16 v[196:199], a[68:71], a[104:107], v[196:199]\n"
    "v_exp_f32 v27, v187\n"
    "v_mfma_f32_16x16x32_bf16 v[196:199], a[84:87], a[108:111], v[196:199]\n"
    "v_exp_f32 v28, v188\n"
    "v_exp_f32 v29, v189\n"
    "v_mfma_f32_16x16x32_bf16 v[196:199], a[4:7], a[120:123], v[196:199]\n"
    "v_exp_f32 v30, v190\n"
    "v_mfma_f32_16x16x32_bf16 v[196:199], a[20:23], a[124:127], v[196:199]\n"
    "v_exp_f32 v31, v191\n"
    "v_mfma_f32_16x16x32_bf16 v[200:203], a[40:43], a[96:99], v[200:203]\n"
    "v_rcp_f32 v8, v8\n"
    "v_mfma_f32_16x16x32_bf16 v[200:203], a[56:59], a[100:103], v[200:203]\n"
    "v_rcp_f32 v9, v9\n"
    "v_rcp_f32 v10, v10\n"
    "v_mfma_f32_16x16x32_bf16 v[200:203], a[72:75], a[104:107], v[200:203]\n"
    "v_rcp_f32 v11, v11\n"
    "v_mfma_f32_16x16x32_bf16 v[200:203], a[88:91], a[108:111], v[200:203]\n"
    "v_rcp_f32 v12, v12\n"
    "v_mfma_f32_16x16x32_bf16 v[200:203], a[8:11], a[120:123], v[200:203]\n"
    "v_rcp_f32 v13, v13\n"
    "v_mfma_f32_16x16x32_bf16 v[200:203], a[24:27], a[124:127], v[200:203]\n"
    "v_rcp_f32 v14, v14\n"
    "v_rcp_f32 v15, v15\n"
    "v_pk_fma_f32 v[192:193], v[192:193], v[32:33], v[48:49]\n"
    "v_pk_fma_f32 v[194:195], v[194:195], v[34:35], v[50:51]\n"
    "v_pk_fma_f32 v[196:197], v[196:197], v[36:37], v[52:53]\n"
    "v_pk_fma_f32 v[198:199], v[198:199], v[38:39], v[54:55]\n"
    "v_pk_mul_f32 v[176:177], v[176:177], v[8:9]\n"
    "v_pk_mul_f32 v[178:179], v[178:179], v[10:11]\n"
    "v_pk_mul_f32 v[180:181], v[180:181], v[12:13]\n"
    "v_pk_mul_f32 v[182:183], v[182:183], v[14:15]\n"
    "v_pk_fma_f32 v[24:25], v[24:25], s[54:55], s[54:55]\n"
    "v_pk_fma_f32 v[26:27], v[26:27], s[54:55], s[54:55]\n"
    "v_pk_fma_f32 v[28:29], v[28:29], s[54:55], s[54:55]\n"
    "v_pk_fma_f32 v[30:31], v[30:31], s[54:55], s[54:55]\n"
    "v_cvt_pk_bf16_f32 v176, v176, v177\n"
    "v_cvt_pk_bf16_f32 v177, v178, v179\n"
    "v_exp_f32 v16, v192\n"
    "v_mfma_f32_16x16x32_bf16 v[204:207], a[44:47], a[96:99], v[204:207]\n"
    "v_cvt_pk_bf16_f32 v178, v180, v181\n"
    "v_cvt_pk_bf16_f32 v179, v182, v183\n"
    "buffer_store_dwordx4 v[176:179], v1, s[64:67], s51 offen offset:0 sc1\n"
    "v_exp_f32 v17, v193\n"
    "v_mfma_f32_16x16x32_bf16 v[204:207], a[60:63], a[100:103], v[204:207]\n"
    "v_exp_f32 v18, v194\n"
    "v_mfma_f32_16x16x32_bf16 v[204:207], a[76:79], a[104:107], v[204:207]\n"
    "v_exp_f32 v19, v195\n"
    "v_mfma_f32_16x16x32_bf16 v[204:207], a[92:95], a[108:111], v[204:207]\n"
    "v_exp_f32 v20, v196\n"
    "v_exp_f32 v21, v197\n"
    "v_mfma_f32_16x16x32_bf16 v[204:207], a[12:15], a[120:123], v[204:207]\n"
    "v_exp_f32 v22, v198\n"
    "v_mfma_f32_16x16x32_bf16 v[204:207], a[28:31], a[124:127], v[204:207]\n"
    "v_exp_f32 v23, v199\n"
    "v_mfma_f32_16x16x32_bf16 v[208:211], a[32:35], a[104:107], v[208:211]\n"
    "v_rcp_f32 v24, v24\n"
    "v_mfma_f32_16x16x32_bf16 v[208:211], a[48:51], a[108:111], v[208:211]\n"
    "ds_read_b128 a[120:123], v2 offset:384\n"
    "v_rcp_f32 v25, v25\n"
    "v_rcp_f32 v26, v26\n"
    "s_waitcnt lgkmcnt(1)\n"
    "v_mfma_f32_16x16x32_bf16 v[208:211], a[64:67], a[112:115], v[208:211]\n"
    "v_rcp_f32 v27, v27\n"
    "v_mfma_f32_16x16x32_bf16 v[208:211], a[80:83], a[116:119], v[208:211]\n"
    "ds_read_b128 a[124:127], v2 offset:448\n"
    "v_rcp_f32 v28, v28\n"
    "v_mfma_f32_16x16x32_bf16 v[208:211], a[0:3], a[96:99], v[208:211]\n"
    "v_rcp_f32 v29, v29\n"
    "v_mfma_f32_16x16x32_bf16 v[208:211], a[16:19], a[100:103], v[208:211]\n"
    "v_rcp_f32 v30, v30\n"
    "v_rcp_f32 v31, v31\n"
    "v_pk_fma_f32 v[200:201], v[200:201], v[40:41], v[56:57]\n"
    "v_pk_fma_f32 v[202:203], v[202:203], v[42:43], v[58:59]\n"
    "v_pk_fma_f32 v[204:205], v[204:205], v[44:45], v[60:61]\n"
    "v_pk_fma_f32 v[206:207], v[206:207], v[46:47], v[62:63]\n"
    "v_pk_mul_f32 v[184:185], v[184:185], v[24:25]\n"
    "v_pk_mul_f32 v[186:187], v[186:187], v[26:27]\n"
    "v_pk_mul_f32 v[188:189], v[188:189], v[28:29]\n"
    "v_pk_mul_f32 v[190:191], v[190:191], v[30:31]\n"
    "v_pk_fma_f32 v[16:17], v[16:17], s[54:55], s[54:55]\n"
    "v_pk_fma_f32 v[18:19], v[18:19], s[54:55], s[54:55]\n"
    "v_pk_fma_f32 v[20:21], v[20:21], s[54:55], s[54:55]\n"
    "v_pk_fma_f32 v[22:23], v[22:23], s[54:55], s[54:55]\n"
    "v_cvt_pk_bf16_f32 v184, v184, v185\n"
    "v_cvt_pk_bf16_f32 v185, v186, v187\n"
    "v_exp_f32 v8, v200\n"
    "v_mfma_f32_16x16x32_bf16 v[212:215], a[36:39], a[104:107], v[212:215]\n"
    "v_cvt_pk_bf16_f32 v186, v188, v189\n"
    "v_cvt_pk_bf16_f32 v187, v190, v191\n"
    "buffer_store_dwordx4 v[184:187], v1, s[64:67], s51 offen offset:64 sc1\n"
    "s_add_u32 s51, s51, s52\n"
    "v_exp_f32 v9, v201\n"
    "v_mfma_f32_16x16x32_bf16 v[212:215], a[52:55], a[108:111], v[212:215]\n"
    "v_exp_f32 v10, v202\n"
    "v_mfma_f32_16x16x32_bf16 v[212:215], a[68:71], a[112:115], v[212:215]\n"
    "v_exp_f32 v11, v203\n"
    "v_mfma_f32_16x16x32_bf16 v[212:215], a[84:87], a[116:119], v[212:215]\n"
    "v_exp_f32 v12, v204\n"
    "v_exp_f32 v13, v205\n"
    "v_mfma_f32_16x16x32_bf16 v[212:215], a[4:7], a[96:99], v[212:215]\n"
    "v_exp_f32 v14, v206\n"
    "v_mfma_f32_16x16x32_bf16 v[212:215], a[20:23], a[100:103], v[212:215]\n"
    "v_exp_f32 v15, v207\n"
    "v_mfma_f32_16x16x32_bf16 v[216:219], a[40:43], a[104:107], v[216:219]\n"
    "v_rcp_f32 v16, v16\n"
    "v_mfma_f32_16x16x32_bf16 v[216:219], a[56:59], a[108:111], v[216:219]\n"
    "v_rcp_f32 v17, v17\n"
    "v_rcp_f32 v18, v18\n"
    "v_mfma_f32_16x16x32_bf16 v[216:219], a[72:75], a[112:115], v[216:219]\n"
    "v_rcp_f32 v19, v19\n"
    "v_mfma_f32_16x16x32_bf16 v[216:219], a[88:91], a[116:119], v[216:219]\n"
    "v_rcp_f32 v20, v20\n"
    "v_mfma_f32_16x16x32_bf16 v[216:219], a[8:11], a[96:99], v[216:219]\n"
    "v_rcp_f32 v21, v21\n"
    "v_mfma_f32_16x16x32_bf16 v[216:219], a[24:27], a[100:103], v[216:219]\n"
    "v_rcp_f32 v22, v22\n"
    "v_rcp_f32 v23, v23\n"
    "v_pk_fma_f32 v[208:209], v[208:209], v[32:33], v[48:49]\n"
    "v_pk_fma_f32 v[210:211], v[210:211], v[34:35], v[50:51]\n"
    "v_pk_fma_f32 v[212:213], v[212:213], v[36:37], v[52:53]\n"
    "v_pk_fma_f32 v[214:215], v[214:215], v[38:39], v[54:55]\n"
    "v_pk_mul_f32 v[192:193], v[192:193], v[16:17]\n"
    "v_pk_mul_f32 v[194:195], v[194:195], v[18:19]\n"
    "v_pk_mul_f32 v[196:197], v[196:197], v[20:21]\n"
    "v_pk_mul_f32 v[198:199], v[198:199], v[22:23]\n"
    "v_pk_fma_f32 v[8:9], v[8:9], s[54:55], s[54:55]\n"
    "v_pk_fma_f32 v[10:11], v[10:11], s[54:55], s[54:55]\n"
    "v_pk_fma_f32 v[12:13], v[12:13], s[54:55], s[54:55]\n"
    "v_pk_fma_f32 v[14:15], v[14:15], s[54:55], s[54:55]\n"
    "v_cvt_pk_bf16_f32 v192, v192, v193\n"
    "v_cvt_pk_bf16_f32 v193, v194, v195\n"
    "v_exp_f32 v24, v208\n"
    "v_mfma_f32_16x16x32_bf16 v[220:223], a[44:47], a[104:107], v[220:223]\n"
    "v_cvt_pk_bf16_f32 v194, v196, v197\n"
    "v_cvt_pk_bf16_f32 v195, v198, v199\n"
    "buffer_store_dwordx4 v[192:195], v1, s[64:67], s51 offen offset:0 sc1\n"
    "v_exp_f32 v25, v209\n"
    "v_mfma_f32_16x16x32_bf16 v[220:223], a[60:63], a[108:111], v[220:223]\n"
    "v_exp_f32 v26, v210\n"
    "v_mfma_f32_16x16x32_bf16 v[220:223], a[76:79], a[112:115], v[220:223]\n"
    "v_exp_f32 v27, v211\n"
    "v_mfma_f32_16x16x32_bf16 v[220:223], a[92:95], a[116:119], v[220:223]\n"
    "v_exp_f32 v28, v212\n"
    "v_exp_f32 v29, v213\n"
    "v_mfma_f32_16x16x32_bf16 v[220:223], a[12:15], a[96:99], v[220:223]\n"
    "v_exp_f32 v30, v214\n"
    "v_mfma_f32_16x16x32_bf16 v[220:223], a[28:31], a[100:103], v[220:223]\n"
    "v_exp_f32 v31, v215\n"
    "v_mfma_f32_16x16x32_bf16 v[224:227], a[32:35], a[112:115], v[224:227]\n"
    "v_rcp_f32 v8, v8\n"
    "v_mfma_f32_16x16x32_bf16 v[224:227], a[48:51], a[116:119], v[224:227]\n"
    "ds_read_b128 a[96:99], v2 offset:512\n"
    "v_rcp_f32 v9, v9\n"
    "v_rcp_f32 v10, v10\n"
    "s_waitcnt lgkmcnt(1)\n"
    "v_mfma_f32_16x16x32_bf16 v[224:227], a[64:67], a[120:123], v[224:227]\n"
    "v_rcp_f32 v11, v11\n"
    "v_mfma_f32_16x16x32_bf16 v[224:227], a[80:83], a[124:127], v[224:227]\n"
    "ds_read_b128 a[100:103], v2 offset:576\n"
    "v_rcp_f32 v12, v12\n"
    "v_mfma_f32_16x16x32_bf16 v[224:227], a[0:3], a[104:107], v[224:227]\n"
    "v_rcp_f32 v13, v13\n"
    "v_mfma_f32_16x16x32_bf16 v[224:227], a[16:19], a[108:111], v[224:227]\n"
    "v_rcp_f32 v14, v14\n"
    "v_rcp_f32 v15, v15\n"
    "v_pk_fma_f32 v[216:217], v[216:217], v[40:41], v[56:57]\n"
    "v_pk_fma_f32 v[218:219], v[218:219], v[42:43], v[58:59]\n"
    "v_pk_fma_f32 v[220:221], v[220:221], v[44:45], v[60:61]\n"
    "v_pk_fma_f32 v[222:223], v[222:223], v[46:47], v[62:63]\n"
    "v_pk_mul_f32 v[200:201], v[200:201], v[8:9]\n"
    "v_pk_mul_f32 v[202:203], v[202:203], v[10:11]\n"
    "v_pk_mul_f32 v[204:205], v[204:205], v[12:13]\n"
    "v_pk_mul_f32 v[206:207], v[206:207], v[14:15]\n"
    "v_pk_fma_f32 v[24:25], v[24:25], s[54:55], s[54:55]\n"
    "v_pk_fma_f32 v[26:27], v[26:27], s[54:55], s[54:55]\n"
    "v_pk_fma_f32 v[28:29], v[28:29], s[54:55], s[54:55]\n"
    "v_pk_fma_f32 v[30:31], v[30:31], s[54:55], s[54:55]\n"
    "v_cvt_pk_bf16_f32 v200, v200, v201\n"
    "v_cvt_pk_bf16_f32 v201, v202, v203\n"
    "v_exp_f32 v16, v216\n"
    "v_mfma_f32_16x16x32_bf16 v[228:231], a[36:39], a[112:115], v[228:231]\n"
    "v_cvt_pk_bf16_f32 v202, v204, v205\n"
    "v_cvt_pk_bf16_f32 v203, v206, v207\n"
    "buffer_store_dwordx4 v[200:203], v1, s[64:67], s51 offen offset:64 sc1\n"
    "s_add_u32 s51, s51, s52\n"
    "v_exp_f32 v17, v217\n"
    "v_mfma_f32_16x16x32_bf16 v[228:231], a[52:55], a[116:119], v[228:231]\n"
    "v_exp_f32 v18, v218\n"
    "v_mfma_f32_16x16x32_bf16 v[228:231], a[68:71], a[120:123], v[228:231]\n"
    "v_exp_f32 v19, v219\n"
    "v_mfma_f32_16x16x32_bf16 v[228:231], a[84:87], a[124:127], v[228:231]\n"
    "v_exp_f32 v20, v220\n"
    "v_exp_f32 v21, v221\n"
    "v_mfma_f32_16x16x32_bf16 v[228:231], a[4:7], a[104:107], v[228:231]\n"
    "v_exp_f32 v22, v222\n"
    "v_mfma_f32_16x16x32_bf16 v[228:231], a[20:23], a[108:111], v[228:231]\n"
    "v_exp_f32 v23, v223\n"
    "v_mfma_f32_16x16x32_bf16 v[232:235], a[40:43], a[112:115], v[232:235]\n"
    "v_rcp_f32 v24, v24\n"
    "v_mfma_f32_16x16x32_bf16 v[232:235], a[56:59], a[116:119], v[232:235]\n"
    "v_rcp_f32 v25, v25\n"
    "v_rcp_f32 v26, v26\n"
    "v_mfma_f32_16x16x32_bf16 v[232:235], a[72:75], a[120:123], v[232:235]\n"
    "v_rcp_f32 v27, v27\n"
    "v_mfma_f32_16x16x32_bf16 v[232:235], a[88:91], a[124:127], v[232:235]\n"
    "v_rcp_f32 v28, v28\n"
    "v_mfma_f32_16x16x32_bf16 v[232:235], a[8:11], a[104:107], v[232:235]\n"
    "v_rcp_f32 v29, v29\n"
    "v_mfma_f32_16x16x32_bf16 v[232:235], a[24:27], a[108:111], v[232:235]\n"
    "v_rcp_f32 v30, v30\n"
    "v_rcp_f32 v31, v31\n"
    "v_pk_fma_f32 v[224:225], v[224:225], v[32:33], v[48:49]\n"
    "v_pk_fma_f32 v[226:227], v[226:227], v[34:35], v[50:51]\n"
    "v_pk_fma_f32 v[228:229], v[228:229], v[36:37], v[52:53]\n"
    "v_pk_fma_f32 v[230:231], v[230:231], v[38:39], v[54:55]\n"
    "v_pk_mul_f32 v[208:209], v[208:209], v[24:25]\n"
    "v_pk_mul_f32 v[210:211], v[210:211], v[26:27]\n"
    "v_pk_mul_f32 v[212:213], v[212:213], v[28:29]\n"
    "v_pk_mul_f32 v[214:215], v[214:215], v[30:31]\n"
    "v_pk_fma_f32 v[16:17], v[16:17], s[54:55], s[54:55]\n"
    "v_pk_fma_f32 v[18:19], v[18:19], s[54:55], s[54:55]\n"
    "v_pk_fma_f32 v[20:21], v[20:21], s[54:55], s[54:55]\n"
    "v_pk_fma_f32 v[22:23], v[22:23], s[54:55], s[54:55]\n"
    "v_cvt_pk_bf16_f32 v208, v208, v209\n"
    "v_cvt_pk_bf16_f32 v209, v210, v211\n"
    "v_exp_f32 v8, v224\n"
    "v_mfma_f32_16x16x32_bf16 v[236:239], a[44:47], a[112:115], v[236:239]\n"
    "v_cvt_pk_bf16_f32 v210, v212, v213\n"
    "v_cvt_pk_bf16_f32 v211, v214, v215\n"
    "buffer_store_dwordx4 v[208:211], v1, s[64:67], s51 offen offset:0 sc1\n"
    "v_exp_f32 v9, v225\n"
    "v_mfma_f32_16x16x32_bf16 v[236:239], a[60:63], a[116:119], v[236:239]\n"
    "v_exp_f32 v10, v226\n"
    "v_mfma_f32_16x16x32_bf16 v[236:239], a[76:79], a[120:123], v[236:239]\n"
    "v_exp_f32 v11, v227\n"
    "v_mfma_f32_16x16x32_bf16 v[236:239], a[92:95], a[124:127], v[236:239]\n"
    "v_exp_f32 v12, v228\n"
    "v_exp_f32 v13, v229\n"
    "v_mfma_f32_16x16x32_bf16 v[236:239], a[12:15], a[104:107], v[236:239]\n"
    "v_exp_f32 v14, v230\n"
    "v_mfma_f32_16x16x32_bf16 v[236:239], a[28:31], a[108:111], v[236:239]\n"
    "v_exp_f32 v15, v231\n"
    "v_mfma_f32_16x16x32_bf16 v[240:243], a[32:35], a[120:123], v[240:243]\n"
    "v_rcp_f32 v16, v16\n"
    "v_mfma_f32_16x16x32_bf16 v[240:243], a[48:51], a[124:127], v[240:243]\n"
    "ds_read_b128 a[104:107], v2 offset:640\n"
    "v_rcp_f32 v17, v17\n"
    "v_rcp_f32 v18, v18\n"
    "s_waitcnt lgkmcnt(1)\n"
    "v_mfma_f32_16x16x32_bf16 v[240:243], a[64:67], a[96:99], v[240:243]\n"
    "v_rcp_f32 v19, v19\n"
    "v_mfma_f32_16x16x32_bf16 v[240:243], a[80:83], a[100:103], v[240:243]\n"
    "ds_read_b128 a[108:111], v2 offset:704\n"
    "v_rcp_f32 v20, v20\n"
    "v_mfma_f32_16x16x32_bf16 v[240:243], a[0:3], a[112:115], v[240:243]\n"
    "v_rcp_f32 v21, v21\n"
    "v_mfma_f32_16x16x32_bf16 v[240:243], a[16:19], a[116:119], v[240:243]\n"
    "v_rcp_f32 v22, v22\n"
    "v_rcp_f32 v23, v23\n"
    "v_pk_fma_f32 v[232:233], v[232:233], v[40:41], v[56:57]\n"
    "v_pk_fma_f32 v[234:235], v[234:235], v[42:43], v[58:59]\n"
    "v_pk_fma_f32 v[236:237], v[236:237], v[44:45], v[60:61]\n"
    "v_pk_fma_f32 v[238:239], v[238:239], v[46:47], v[62:63]\n"
    "v_pk_mul_f32 v[216:217], v[216:217], v[16:17]\n"
    "v_pk_mul_f32 v[218:219], v[218:219], v[18:19]\n"
    "v_pk_mul_f32 v[220:221], v[220:221], v[20:21]\n"
    "v_pk_mul_f32 v[222:223], v[222:223], v[22:23]\n"
    "v_pk_fma_f32 v[8:9], v[8:9], s[54:55], s[54:55]\n"
    "v_pk_fma_f32 v[10:11], v[10:11], s[54:55], s[54:55]\n"
    "v_pk_fma_f32 v[12:13], v[12:13], s[54:55], s[54:55]\n"
    "v_pk_fma_f32 v[14:15], v[14:15], s[54:55], s[54:55]\n"
    "v_cvt_pk_bf16_f32 v216, v216, v217\n"
    "v_cvt_pk_bf16_f32 v217, v218, v219\n"
    "v_exp_f32 v24, v232\n"
    "v_mfma_f32_16x16x32_bf16 v[244:247], a[36:39], a[120:123], v[244:247]\n"
    "v_cvt_pk_bf16_f32 v218, v220, v221\n"
    "v_cvt_pk_bf16_f32 v219, v222, v223\n"
    "buffer_store_dwordx4 v[216:219], v1, s[64:67], s51 offen offset:64 sc1\n"
    "s_add_u32 s51, s51, s52\n"
    "v_exp_f32 v25, v233\n"
    "v_mfma_f32_16x16x32_bf16 v[244:247], a[52:55], a[124:127], v[244:247]\n"
    "v_exp_f32 v26, v234\n"
    "v_mfma_f32_16x16x32_bf16 v[244:247], a[68:71], a[96:99], v[244:247]\n"
    "v_exp_f32 v27, v235\n"
    "v_mfma_f32_16x16x32_bf16 v[244:247], a[84:87], a[100:103], v[244:247]\n"
    "v_exp_f32 v28, v236\n"
    "v_exp_f32 v29, v237\n"
    "v_mfma_f32_16x16x32_bf16 v[244:247], a[4:7], a[112:115], v[244:247]\n"
    "v_exp_f32 v30, v238\n"
    "v_mfma_f32_16x16x32_bf16 v[244:247], a[20:23], a[116:119], v[244:247]\n"
    "v_exp_f32 v31, v239\n"
    "v_mfma_f32_16x16x32_bf16 v[248:251], a[40:43], a[120:123], v[248:251]\n"
    "v_rcp_f32 v8, v8\n"
    "v_mfma_f32_16x16x32_bf16 v[248:251], a[56:59], a[124:127], v[248:251]\n"
    "v_rcp_f32 v9, v9\n"
    "v_rcp_f32 v10, v10\n"
    "v_mfma_f32_16x16x32_bf16 v[248:251], a[72:75], a[96:99], v[248:251]\n"
    "v_rcp_f32 v11, v11\n"
    "v_mfma_f32_16x16x32_bf16 v[248:251], a[88:91], a[100:103], v[248:251]\n"
    "v_rcp_f32 v12, v12\n"
    "v_mfma_f32_16x16x32_bf16 v[248:251], a[8:11], a[112:115], v[248:251]\n"
    "v_rcp_f32 v13, v13\n"
    "v_mfma_f32_16x16x32_bf16 v[248:251], a[24:27], a[116:119], v[248:251]\n"
    "v_rcp_f32 v14, v14\n"
    "v_rcp_f32 v15, v15\n"
    "v_pk_fma_f32 v[240:241], v[240:241], v[32:33], v[48:49]\n"
    "v_pk_fma_f32 v[242:243], v[242:243], v[34:35], v[50:51]\n"
    "v_pk_fma_f32 v[244:245], v[244:245], v[36:37], v[52:53]\n"
    "v_pk_fma_f32 v[246:247], v[246:247], v[38:39], v[54:55]\n"
    "v_pk_mul_f32 v[224:225], v[224:225], v[8:9]\n"
    "v_pk_mul_f32 v[226:227], v[226:227], v[10:11]\n"
    "v_pk_mul_f32 v[228:229], v[228:229], v[12:13]\n"
    "v_pk_mul_f32 v[230:231], v[230:231], v[14:15]\n"
    "v_pk_fma_f32 v[24:25], v[24:25], s[54:55], s[54:55]\n"
    "v_pk_fma_f32 v[26:27], v[26:27], s[54:55], s[54:55]\n"
    "v_pk_fma_f32 v[28:29], v[28:29], s[54:55], s[54:55]\n"
    "v_pk_fma_f32 v[30:31], v[30:31], s[54:55], s[54:55]\n"
    "v_cvt_pk_bf16_f32 v224, v224, v225\n"
    "v_cvt_pk_bf16_f32 v225, v226, v227\n"
    "v_exp_f32 v16, v240\n"
    "v_mfma_f32_16x16x32_bf16 v[252:255], a[44:47], a[120:123], v[252:255]\n"
    "v_cvt_pk_bf16_f32 v226, v228, v229\n"
    "v_cvt_pk_bf16_f32 v227, v230, v231\n"
    "buffer_store_dwordx4 v[224:227], v1, s[64:67], s51 offen offset:0 sc1\n"
    "v_exp_f32 v17, v241\n"
    "v_mfma_f32_16x16x32_bf16 v[252:255], a[60:63], a[124:127], v[252:255]\n"
    "v_exp_f32 v18, v242\n"
    "v_mfma_f32_16x16x32_bf16 v[252:255], a[76:79], a[96:99], v[252:255]\n"
    "v_exp_f32 v19, v243\n"
    "v_mfma_f32_16x16x32_bf16 v[252:255], a[92:95], a[100:103], v[252:255]\n"
    "v_exp_f32 v20, v244\n"
    "v_exp_f32 v21, v245\n"
    "v_mfma_f32_16x16x32_bf16 v[252:255], a[12:15], a[112:115], v[252:255]\n"
    "v_exp_f32 v22, v246\n"
    "v_mfma_f32_16x16x32_bf16 v[252:255], a[28:31], a[116:119], v[252:255]\n"
    "v_exp_f32 v23, v247\n"
    "v_mfma_f32_16x16x32_bf16 a[136:139], a[32:35], a[96:99], a[136:139]\n"
    "v_rcp_f32 v24, v24\n"
    "v_mfma_f32_16x16x32_bf16 a[136:139], a[48:51], a[100:103], a[136:139]\n"
    "ds_read_b128 a[112:115], v2 offset:768\n"
    "v_rcp_f32 v25, v25\n"
    "v_rcp_f32 v26, v26\n"
    "s_waitcnt lgkmcnt(1)\n"
    "v_mfma_f32_16x16x32_bf16 a[136:139], a[64:67], a[104:107], a[136:139]\n"
    "v_rcp_f32 v27, v27\n"
    "v_mfma_f32_16x16x32_bf16 a[136:139], a[80:83], a[108:111], a[136:139]\n"
    "ds_read_b128 a[116:119], v2 offset:832\n"
    "v_rcp_f32 v28, v28\n"
    "v_mfma_f32_16x16x32_bf16 a[136:139], a[0:3], a[120:123], a[136:139]\n"
    "v_rcp_f32 v29, v29\n"
    "v_mfma_f32_16x16x32_bf16 a[136:139], a[16:19], a[124:127], a[136:139]\n"
    "v_rcp_f32 v30, v30\n"
    "v_rcp_f32 v31, v31\n"
    "v_pk_fma_f32 v[248:249], v[248:249], v[40:41], v[56:57]\n"
    "v_pk_fma_f32 v[250:251], v[250:251], v[42:43], v[58:59]\n"
    "v_pk_fma_f32 v[252:253], v[252:253], v[44:45], v[60:61]\n"
    "v_pk_fma_f32 v[254:255], v[254:255], v[46:47], v[62:63]\n"
    "v_pk_mul_f32 v[232:233], v[232:233], v[24:25]\n"
    "v_pk_mul_f32 v[234:235], v[234:235], v[26:27]\n"
    "v_pk_mul_f32 v[236:237], v[236:237], v[28:29]\n"
    "v_pk_mul_f32 v[238:239], v[238:239], v[30:31]\n"
    "v_pk_fma_f32 v[16:17], v[16:17], s[54:55], s[54:55]\n"
    "v_pk_fma_f32 v[18:19], v[18:19], s[54:55], s[54:55]\n"
    "v_pk_fma_f32 v[20:21], v[20:21], s[54:55], s[54:55]\n"
    "v_pk_fma_f32 v[22:23], v[22:23], s[54:55], s[54:55]\n"
    "v_cvt_pk_bf16_f32 v232, v232, v233\n"
    "v_cvt_pk_bf16_f32 v233, v234, v235\n"
    "v_exp_f32 v8, v248\n"
    "v_mfma_f32_16x16x32_bf16 a[140:143], a[36:39], a[96:99], a[140:143]\n"
    "v_cvt_pk_bf16_f32 v234, v236, v237\n"
    "v_cvt_pk_bf16_f32 v235, v238, v239\n"
    "buffer_store_dwordx4 v[232:235], v1, s[64:67], s51 offen offset:64 sc1\n"
    "s_add_u32 s51, s51, s52\n"
    "v_exp_f32 v9, v249\n"
    "v_mfma_f32_16x16x32_bf16 a[140:143], a[52:55], a[100:103], a[140:143]\n"
    "v_exp_f32 v10, v250\n"
    "v_mfma_f32_16x16x32_bf16 a[140:143], a[68:71], a[104:107], a[140:143]\n"
    "v_exp_f32 v11, v251\n"
    "v_mfma_f32_16x16x32_bf16 a[140:143], a[84:87], a[108:111], a[140:143]\n"
    "v_exp_f32 v12, v252\n"
    "v_exp_f32 v13, v253\n"
    "v_mfma_f32_16x16x32_bf16 a[140:143], a[4:7], a[120:123], a[140:143]\n"
    "v_exp_f32 v14, v254\n"
    "v_mfma_f32_16x16x32_bf16 a[140:143], a[20:23], a[124:127], a[140:143]\n"
    "v_exp_f32 v15, v255\n"
    "v_mfma_f32_16x16x32_bf16 a[144:147], a[40:43], a[96:99], a[144:147]\n"
    "v_rcp_f32 v16, v16\n"
    "v_mfma_f32_16x16x32_bf16 a[144:147], a[56:59], a[100:103], a[144:147]\n"
    "v_rcp_f32 v17, v17\n"
    "v_rcp_f32 v18, v18\n"
    "v_mfma_f32_16x16x32_bf16 a[144:147], a[72:75], a[104:107], a[144:147]\n"
    "v_rcp_f32 v19, v19\n"
    "v_mfma_f32_16x16x32_bf16 a[144:147], a[88:91], a[108:111], a[144:147]\n"
    "v_rcp_f32 v20, v20\n"
    "v_mfma_f32_16x16x32_bf16 a[144:147], a[8:11], a[120:123], a[144:147]\n"
    "v_rcp_f32 v21, v21\n"
    "v_mfma_f32_16x16x32_bf16 a[144:147], a[24:27], a[124:127], a[144:147]\n"
    "v_rcp_f32 v22, v22\n"
    "v_rcp_f32 v23, v23\n"
    "v_accvgpr_read_b32 v64, a136\n"
    "v_accvgpr_read_b32 v65, a137\n"
    "v_accvgpr_read_b32 v66, a138\n"
    "v_accvgpr_read_b32 v67, a139\n"
    "v_accvgpr_read_b32 v68, a140\n"
    "v_accvgpr_read_b32 v69, a141\n"
    "v_accvgpr_read_b32 v70, a142\n"
    "v_accvgpr_read_b32 v71, a143\n"
    "v_pk_fma_f32 v[64:65], v[64:65], v[32:33], v[48:49]\n"
    "v_pk_fma_f32 v[66:67], v[66:67], v[34:35], v[50:51]\n"
    "v_pk_fma_f32 v[68:69], v[68:69], v[36:37], v[52:53]\n"
    "v_pk_fma_f32 v[70:71], v[70:71], v[38:39], v[54:55]\n"
    "v_pk_mul_f32 v[240:241], v[240:241], v[16:17]\n"
    "v_pk_mul_f32 v[242:243], v[242:243], v[18:19]\n"
    "v_pk_mul_f32 v[244:245], v[244:245], v[20:21]\n"
    "v_pk_mul_f32 v[246:247], v[246:247], v[22:23]\n"
    "v_pk_fma_f32 v[8:9], v[8:9], s[54:55], s[54:55]\n"
    "v_pk_fma_f32 v[10:11], v[10:11], s[54:55], s[54:55]\n"
    "v_pk_fma_f32 v[12:13], v[12:13], s[54:55], s[54:55]\n"
    "v_pk_fma_f32 v[14:15], v[14:15], s[54:55], s[54:55]\n"
    "v_cvt_pk_bf16_f32 v240, v240, v241\n"
    "v_cvt_pk_bf16_f32 v241, v242, v243\n"
    "v_exp_f32 v24, v64\n"
    "v_mfma_f32_16x16x32_bf16 a[148:151], a[44:47], a[96:99], a[148:151]\n"
    "v_cvt_pk_bf16_f32 v242, v244, v245\n"
    "v_cvt_pk_bf16_f32 v243, v246, v247\n"
    "buffer_store_dwordx4 v[240:243], v1, s[64:67], s51 offen offset:0 sc1\n"
    "v_exp_f32 v25, v65\n"
    "v_mfma_f32_16x16x32_bf16 a[148:151], a[60:63], a[100:103], a[148:151]\n"
    "v_exp_f32 v26, v66\n"
    "v_mfma_f32_16x16x32_bf16 a[148:151], a[76:79], a[104:107], a[148:151]\n"
    "v_exp_f32 v27, v67\n"
    "v_mfma_f32_16x16x32_bf16 a[148:151], a[92:95], a[108:111], a[148:151]\n"
    "v_exp_f32 v28, v68\n"
    "v_exp_f32 v29, v69\n"
    "v_mfma_f32_16x16x32_bf16 a[148:151], a[12:15], a[120:123], a[148:151]\n"
    "v_exp_f32 v30, v70\n"
    "v_mfma_f32_16x16x32_bf16 a[148:151], a[28:31], a[124:127], a[148:151]\n"
    "v_exp_f32 v31, v71\n"
    "v_mfma_f32_16x16x32_bf16 a[152:155], a[32:35], a[104:107], a[152:155]\n"
    "v_rcp_f32 v8, v8\n"
    "v_mfma_f32_16x16x32_bf16 a[152:155], a[48:51], a[108:111], a[152:155]\n"
    "ds_read_b128 a[120:123], v2 offset:896\n"
    "v_rcp_f32 v9, v9\n"
    "v_rcp_f32 v10, v10\n"
    "s_waitcnt lgkmcnt(1)\n"
    "v_mfma_f32_16x16x32_bf16 a[152:155], a[64:67], a[112:115], a[152:155]\n"
    "v_rcp_f32 v11, v11\n"
    "v_mfma_f32_16x16x32_bf16 a[152:155], a[80:83], a[116:119], a[152:155]\n"
    "ds_read_b128 a[124:127], v2 offset:960\n"
    "v_rcp_f32 v12, v12\n"
    "v_mfma_f32_16x16x32_bf16 a[152:155], a[0:3], a[96:99], a[152:155]\n"
    "v_rcp_f32 v13, v13\n"
    "v_mfma_f32_16x16x32_bf16 a[152:155], a[16:19], a[100:103], a[152:155]\n"
    "v_rcp_f32 v14, v14\n"
    "v_rcp_f32 v15, v15\n"
    "v_accvgpr_read_b32 v72, a144\n"
    "v_accvgpr_read_b32 v73, a145\n"
    "v_accvgpr_read_b32 v74, a146\n"
    "v_accvgpr_read_b32 v75, a147\n"
    "v_accvgpr_read_b32 v76, a148\n"
    "v_accvgpr_read_b32 v77, a149\n"
    "v_accvgpr_read_b32 v78, a150\n"
    "v_accvgpr_read_b32 v79, a151\n"
    "v_pk_fma_f32 v[72:73], v[72:73], v[40:41], v[56:57]\n"
    "v_pk_fma_f32 v[74:75], v[74:75], v[42:43], v[58:59]\n"
    "v_pk_fma_f32 v[76:77], v[76:77], v[44:45], v[60:61]\n"
    "v_pk_fma_f32 v[78:79], v[78:79], v[46:47], v[62:63]\n"
    "v_pk_mul_f32 v[248:249], v[248:249], v[8:9]\n"
    "v_pk_mul_f32 v[250:251], v[250:251], v[10:11]\n"
    "v_pk_mul_f32 v[252:253], v[252:253], v[12:13]\n"
    "v_pk_mul_f32 v[254:255], v[254:255], v[14:15]\n"
    "v_pk_fma_f32 v[24:25], v[24:25], s[54:55], s[54:55]\n"
    "v_pk_fma_f32 v[26:27], v[26:27], s[54:55], s[54:55]\n"
    "v_pk_fma_f32 v[28:29], v[28:29], s[54:55], s[54:55]\n"
    "v_pk_fma_f32 v[30:31], v[30:31], s[54:55], s[54:55]\n"
    "v_cvt_pk_bf16_f32 v248, v248, v249\n"
    "v_cvt_pk_bf16_f32 v249, v250, v251\n"
    "v_exp_f32 v16, v72\n"
    "v_mfma_f32_16x16x32_bf16 a[156:159], a[36:39], a[104:107], a[156:159]\n"
    "v_cvt_pk_bf16_f32 v250, v252, v253\n"
    "v_cvt_pk_bf16_f32 v251, v254, v255\n"
    "buffer_store_dwordx4 v[248:251], v1, s[64:67], s51 offen offset:64 sc1\n"
    "s_add_u32 s51, s51, s52\n"
    "v_exp_f32 v17, v73\n"
    "v_mfma_f32_16x16x32_bf16 a[156:159], a[52:55], a[108:111], a[156:159]\n"
    "v_exp_f32 v18, v74\n"
    "v_mfma_f32_16x16x32_bf16 a[156:159], a[68:71], a[112:115], a[156:159]\n"
    "v_exp_f32 v19, v75\n"
    "v_mfma_f32_16x16x32_bf16 a[156:159], a[84:87], a[116:119], a[156:159]\n"
    "v_exp_f32 v20, v76\n"
    "v_exp_f32 v21, v77\n"
    "v_mfma_f32_16x16x32_bf16 a[156:159], a[4:7], a[96:99], a[156:159]\n"
    "v_exp_f32 v22, v78\n"
    "v_mfma_f32_16x16x32_bf16 a[156:159], a[20:23], a[100:103], a[156:159]\n"
    "v_exp_f32 v23, v79\n"
    "v_mfma_f32_16x16x32_bf16 a[160:163], a[40:43], a[104:107], a[160:163]\n"
    "v_rcp_f32 v24, v24\n"
    "v_mfma_f32_16x16x32_bf16 a[160:163], a[56:59], a[108:111], a[160:163]\n"
    "ds_read_b128 a[128:131], v3 offset:896\n"
    "v_rcp_f32 v25, v25\n"
    "v_rcp_f32 v26, v26\n"
    "v_mfma_f32_16x16x32_bf16 a[160:163], a[72:75], a[112:115], a[160:163]\n"
    "v_rcp_f32 v27, v27\n"
    "v_mfma_f32_16x16x32_bf16 a[160:163], a[88:91], a[116:119], a[160:163]\n"
    "ds_read_b128 a[132:135], v3 offset:960\n"
    "v_rcp_f32 v28, v28\n"
    "v_mfma_f32_16x16x32_bf16 a[160:163], a[8:11], a[96:99], a[160:163]\n"
    "v_rcp_f32 v29, v29\n"
    "v_mfma_f32_16x16x32_bf16 a[160:163], a[24:27], a[100:103], a[160:163]\n"
    "v_rcp_f32 v30, v30\n"
    "v_rcp_f32 v31, v31\n"
    "v_accvgpr_read_b32 v80, a152\n"
    "v_accvgpr_read_b32 v81, a153\n"
    "v_accvgpr_read_b32 v82, a154\n"
    "v_accvgpr_read_b32 v83, a155\n"
    "v_accvgpr_read_b32 v84, a156\n"
    "v_accvgpr_read_b32 v85, a157\n"
    "v_accvgpr_read_b32 v86, a158\n"
    "v_accvgpr_read_b32 v87, a159\n"
    "v_pk_fma_f32 v[80:81], v[80:81], v[32:33], v[48:49]\n"
    "v_pk_fma_f32 v[82:83], v[82:83], v[34:35], v[50:51]\n"
    "v_pk_fma_f32 v[84:85], v[84:85], v[36:37], v[52:53]\n"
    "v_pk_fma_f32 v[86:87], v[86:87], v[38:39], v[54:55]\n"
    "v_pk_mul_f32 v[64:65], v[64:65], v[24:25]\n"
    "v_pk_mul_f32 v[66:67], v[66:67], v[26:27]\n"
    "v_pk_mul_f32 v[68:69], v[68:69], v[28:29]\n"
    "v_pk_mul_f32 v[70:71], v[70:71], v[30:31]\n"
    "v_pk_fma_f32 v[16:17], v[16:17], s[54:55], s[54:55]\n"
    "v_pk_fma_f32 v[18:19], v[18:19], s[54:55], s[54:55]\n"
    "v_pk_fma_f32 v[20:21], v[20:21], s[54:55], s[54:55]\n"
    "v_pk_fma_f32 v[22:23], v[22:23], s[54:55], s[54:55]\n"
    "v_cvt_pk_bf16_f32 v64, v64, v65\n"
    "v_cvt_pk_bf16_f32 v65, v66, v67\n"
    "v_exp_f32 v8, v80\n"
    "v_mfma_f32_16x16x32_bf16 a[164:167], a[44:47], a[104:107], a[164:167]\n"
    "v_cvt_pk_bf16_f32 v66, v68, v69\n"
    "v_cvt_pk_bf16_f32 v67, v70, v71\n"
    "buffer_store_dwordx4 v[64:67], v1, s[64:67], s51 offen offset:0 sc1\n"
    "v_exp_f32 v9, v81\n"
    "v_mfma_f32_16x16x32_bf16 a[164:167], a[60:63], a[108:111], a[164:167]\n"
    "v_exp_f32 v10, v82\n"
    "v_mfma_f32_16x16x32_bf16 a[164:167], a[76:79], a[112:115], a[164:167]\n"
    "v_exp_f32 v11, v83\n"
    "v_mfma_f32_16x16x32_bf16 a[164:167], a[92:95], a[116:119], a[164:167]\n"
    "v_exp_f32 v12, v84\n"
    "v_exp_f32 v13, v85\n"
    "v_mfma_f32_16x16x32_bf16 a[164:167], a[12:15], a[96:99], a[164:167]\n"
    "v_exp_f32 v14, v86\n"
    "v_mfma_f32_16x16x32_bf16 a[164:167], a[28:31], a[100:103], a[164:167]\n"
    "v_exp_f32 v15, v87\n"
    "v_mfma_f32_16x16x32_bf16 a[168:171], a[32:35], a[112:115], a[168:171]\n"
    "v_rcp_f32 v16, v16\n"
    "v_mfma_f32_16x16x32_bf16 a[168:171], a[48:51], a[116:119], a[168:171]\n"
    "v_rcp_f32 v17, v17\n"
    "v_rcp_f32 v18, v18\n"
    "s_waitcnt lgkmcnt(0)\n"
    "v_mfma_f32_16x16x32_bf16 a[168:171], a[64:67], a[120:123], a[168:171]\n"
    "v_rcp_f32 v19, v19\n"
    "v_mfma_f32_16x16x32_bf16 a[168:171], a[80:83], a[124:127], a[168:171]\n"
    "v_rcp_f32 v20, v20\n"
    "v_mfma_f32_16x16x32_bf16 a[168:171], a[0:3], a[104:107], a[168:171]\n"
    "v_rcp_f32 v21, v21\n"
    "v_mfma_f32_16x16x32_bf16 a[168:171], a[16:19], a[108:111], a[168:171]\n"
    "v_rcp_f32 v22, v22\n"
    "v_rcp_f32 v23, v23\n"
    "v_accvgpr_read_b32 v88, a160\n"
    "v_accvgpr_read_b32 v89, a161\n"
    "v_accvgpr_read_b32 v90, a162\n"
    "v_accvgpr_read_b32 v91, a163\n"
    "v_accvgpr_read_b32 v92, a164\n"
    "v_accvgpr_read_b32 v93, a165\n"
    "v_accvgpr_read_b32 v94, a166\n"
    "v_accvgpr_read_b32 v95, a167\n"
    "v_pk_fma_f32 v[88:89], v[88:89], v[40:41], v[56:57]\n"
    "v_pk_fma_f32 v[90:91], v[90:91], v[42:43], v[58:59]\n"
    "v_pk_fma_f32 v[92:93], v[92:93], v[44:45], v[60:61]\n"
    "v_pk_fma_f32 v[94:95], v[94:95], v[46:47], v[62:63]\n"
    "v_pk_mul_f32 v[72:73], v[72:73], v[16:17]\n"
    "v_pk_mul_f32 v[74:75], v[74:75], v[18:19]\n"
    "v_pk_mul_f32 v[76:77], v[76:77], v[20:21]\n"
    "v_pk_mul_f32 v[78:79], v[78:79], v[22:23]\n"
    "v_pk_fma_f32 v[8:9], v[8:9], s[54:55], s[54:55]\n"
    "v_pk_fma_f32 v[10:11], v[10:11], s[54:55], s[54:55]\n"
    "v_pk_fma_f32 v[12:13], v[12:13], s[54:55], s[54:55]\n"
    "v_pk_fma_f32 v[14:15], v[14:15], s[54:55], s[54:55]\n"
    "v_cvt_pk_bf16_f32 v72, v72, v73\n"
    "v_cvt_pk_bf16_f32 v73, v74, v75\n"
    "v_exp_f32 v24, v88\n"
    "v_mfma_f32_16x16x32_bf16 a[172:175], a[36:39], a[112:115], a[172:175]\n"
    "v_cvt_pk_bf16_f32 v74, v76, v77\n"
    "v_cvt_pk_bf16_f32 v75, v78, v79\n"
    "buffer_store_dwordx4 v[72:75], v1, s[64:67], s51 offen offset:64 sc1\n"
    "s_add_u32 s51, s51, s52\n"
    "v_exp_f32 v25, v89\n"
    "v_mfma_f32_16x16x32_bf16 a[172:175], a[52:55], a[116:119], a[172:175]\n"
    "v_exp_f32 v26, v90\n"
    "v_mfma_f32_16x16x32_bf16 a[172:175], a[68:71], a[120:123], a[172:175]\n"
    "v_exp_f32 v27, v91\n"
    "v_mfma_f32_16x16x32_bf16 a[172:175], a[84:87], a[124:127], a[172:175]\n"
    "v_exp_f32 v28, v92\n"
    "v_exp_f32 v29, v93\n"
    "v_mfma_f32_16x16x32_bf16 a[172:175], a[4:7], a[104:107], a[172:175]\n"
    "v_exp_f32 v30, v94\n"
    "v_mfma_f32_16x16x32_bf16 a[172:175], a[20:23], a[108:111], a[172:175]\n"
    "v_exp_f32 v31, v95\n"
    "v_mfma_f32_16x16x32_bf16 a[176:179], a[40:43], a[112:115], a[176:179]\n"
    "v_rcp_f32 v8, v8\n"
    "v_mfma_f32_16x16x32_bf16 a[176:179], a[56:59], a[116:119], a[176:179]\n"
    "v_rcp_f32 v9, v9\n"
    "v_rcp_f32 v10, v10\n"
    "v_mfma_f32_16x16x32_bf16 a[176:179], a[72:75], a[120:123], a[176:179]\n"
    "v_rcp_f32 v11, v11\n"
    "v_mfma_f32_16x16x32_bf16 a[176:179], a[88:91], a[124:127], a[176:179]\n"
    "v_rcp_f32 v12, v12\n"
    "v_mfma_f32_16x16x32_bf16 a[176:179], a[8:11], a[104:107], a[176:179]\n"
    "v_rcp_f32 v13, v13\n"
    "v_mfma_f32_16x16x32_bf16 a[176:179], a[24:27], a[108:111], a[176:179]\n"
    "v_rcp_f32 v14, v14\n"
    "v_rcp_f32 v15, v15\n"
    "v_accvgpr_read_b32 v96, a168\n"
    "v_accvgpr_read_b32 v97, a169\n"
    "v_accvgpr_read_b32 v98, a170\n"
    "v_accvgpr_read_b32 v99, a171\n"
    "v_accvgpr_read_b32 v100, a172\n"
    "v_accvgpr_read_b32 v101, a173\n"
    "v_accvgpr_read_b32 v102, a174\n"
    "v_accvgpr_read_b32 v103, a175\n"
    "v_pk_fma_f32 v[96:97], v[96:97], v[32:33], v[48:49]\n"
    "v_pk_fma_f32 v[98:99], v[98:99], v[34:35], v[50:51]\n"
    "v_pk_fma_f32 v[100:101], v[100:101], v[36:37], v[52:53]\n"
    "v_pk_fma_f32 v[102:103], v[102:103], v[38:39], v[54:55]\n"
    "v_pk_mul_f32 v[80:81], v[80:81], v[8:9]\n"
    "v_pk_mul_f32 v[82:83], v[82:83], v[10:11]\n"
    "v_pk_mul_f32 v[84:85], v[84:85], v[12:13]\n"
    "v_pk_mul_f32 v[86:87], v[86:87], v[14:15]\n"
    "v_pk_fma_f32 v[24:25], v[24:25], s[54:55], s[54:55]\n"
    "v_pk_fma_f32 v[26:27], v[26:27], s[54:55], s[54:55]\n"
    "v_pk_fma_f32 v[28:29], v[28:29], s[54:55], s[54:55]\n"
    "v_pk_fma_f32 v[30:31], v[30:31], s[54:55], s[54:55]\n"
    "v_cvt_pk_bf16_f32 v80, v80, v81\n"
    "v_cvt_pk_bf16_f32 v81, v82, v83\n"
    "v_exp_f32 v16, v96\n"
    "v_mfma_f32_16x16x32_bf16 a[180:183], a[44:47], a[112:115], a[180:183]\n"
    "v_cvt_pk_bf16_f32 v82, v84, v85\n"
    "v_cvt_pk_bf16_f32 v83, v86, v87\n"
    "buffer_store_dwordx4 v[80:83], v1, s[64:67], s51 offen offset:0 sc1\n"
    "v_exp_f32 v17, v97\n"
    "v_mfma_f32_16x16x32_bf16 a[180:183], a[60:63], a[116:119], a[180:183]\n"
    "v_exp_f32 v18, v98\n"
    "v_mfma_f32_16x16x32_bf16 a[180:183], a[76:79], a[120:123], a[180:183]\n"
    "v_exp_f32 v19, v99\n"
    "v_mfma_f32_16x16x32_bf16 a[180:183], a[92:95], a[124:127], a[180:183]\n"
    "v_exp_f32 v20, v100\n"
    "v_exp_f32 v21, v101\n"
    "v_mfma_f32_16x16x32_bf16 a[180:183], a[12:15], a[104:107], a[180:183]\n"
    "v_exp_f32 v22, v102\n"
    "v_mfma_f32_16x16x32_bf16 a[180:183], a[28:31], a[108:111], a[180:183]\n"
    "v_exp_f32 v23, v103\n"
    "v_mfma_f32_16x16x32_bf16 a[184:187], a[32:35], a[120:123], a[184:187]\n"
    "v_rcp_f32 v24, v24\n"
    "v_mfma_f32_16x16x32_bf16 a[184:187], a[48:51], a[124:127], a[184:187]\n"
    "v_rcp_f32 v25, v25\n"
    "v_rcp_f32 v26, v26\n"
    "v_mfma_f32_16x16x32_bf16 a[184:187], a[64:67], a[128:131], a[184:187]\n"
    "v_rcp_f32 v27, v27\n"
    "v_mfma_f32_16x16x32_bf16 a[184:187], a[80:83], a[132:135], a[184:187]\n"
    "v_rcp_f32 v28, v28\n"
    "v_mfma_f32_16x16x32_bf16 a[184:187], a[0:3], a[112:115], a[184:187]\n"
    "v_rcp_f32 v29, v29\n"
    "v_mfma_f32_16x16x32_bf16 a[184:187], a[16:19], a[116:119], a[184:187]\n"
    "v_rcp_f32 v30, v30\n"
    "v_rcp_f32 v31, v31\n"
    "v_accvgpr_read_b32 v104, a176\n"
    "v_accvgpr_read_b32 v105, a177\n"
    "v_accvgpr_read_b32 v106, a178\n"
    "v_accvgpr_read_b32 v107, a179\n"
    "v_accvgpr_read_b32 v108, a180\n"
    "v_accvgpr_read_b32 v109, a181\n"
    "v_accvgpr_read_b32 v110, a182\n"
    "v_accvgpr_read_b32 v111, a183\n"
    "v_pk_fma_f32 v[104:105], v[104:105], v[40:41], v[56:57]\n"
    "v_pk_fma_f32 v[106:107], v[106:107], v[42:43], v[58:59]\n"
    "v_pk_fma_f32 v[108:109], v[108:109], v[44:45], v[60:61]\n"
    "v_pk_fma_f32 v[110:111], v[110:111], v[46:47], v[62:63]\n"
    "v_pk_mul_f32 v[88:89], v[88:89], v[24:25]\n"
    "v_pk_mul_f32 v[90:91], v[90:91], v[26:27]\n"
    "v_pk_mul_f32 v[92:93], v[92:93], v[28:29]\n"
    "v_pk_mul_f32 v[94:95], v[94:95], v[30:31]\n"
    "v_pk_fma_f32 v[16:17], v[16:17], s[54:55], s[54:55]\n"
    "v_pk_fma_f32 v[18:19], v[18:19], s[54:55], s[54:55]\n"
    "v_pk_fma_f32 v[20:21], v[20:21], s[54:55], s[54:55]\n"
    "v_pk_fma_f32 v[22:23], v[22:23], s[54:55], s[54:55]\n"
    "v_cvt_pk_bf16_f32 v88, v88, v89\n"
    "v_cvt_pk_bf16_f32 v89, v90, v91\n"
    "v_exp_f32 v8, v104\n"
    "v_mfma_f32_16x16x32_bf16 a[188:191], a[36:39], a[120:123], a[188:191]\n"
    "v_cvt_pk_bf16_f32 v90, v92, v93\n"
    "v_cvt_pk_bf16_f32 v91, v94, v95\n"
    "buffer_store_dwordx4 v[88:91], v1, s[64:67], s51 offen offset:64 sc1\n"
    "s_add_u32 s51, s51, s52\n"
    "v_exp_f32 v9, v105\n"
    "v_mfma_f32_16x16x32_bf16 a[188:191], a[52:55], a[124:127], a[188:191]\n"
    "v_exp_f32 v10, v106\n"
    "v_mfma_f32_16x16x32_bf16 a[188:191], a[68:71], a[128:131], a[188:191]\n"
    "v_exp_f32 v11, v107\n"
    "v_mfma_f32_16x16x32_bf16 a[188:191], a[84:87], a[132:135], a[188:191]\n"
    "v_exp_f32 v12, v108\n"
    "v_exp_f32 v13, v109\n"
    "v_mfma_f32_16x16x32_bf16 a[188:191], a[4:7], a[112:115], a[188:191]\n"
    "v_exp_f32 v14, v110\n"
    "v_mfma_f32_16x16x32_bf16 a[188:191], a[20:23], a[116:119], a[188:191]\n"
    "v_exp_f32 v15, v111\n"
    "v_mfma_f32_16x16x32_bf16 a[192:195], a[40:43], a[120:123], a[192:195]\n"
    "v_rcp_f32 v16, v16\n"
    "v_mfma_f32_16x16x32_bf16 a[192:195], a[56:59], a[124:127], a[192:195]\n"
    "v_rcp_f32 v17, v17\n"
    "v_rcp_f32 v18, v18\n"
    "v_mfma_f32_16x16x32_bf16 a[192:195], a[72:75], a[128:131], a[192:195]\n"
    "v_rcp_f32 v19, v19\n"
    "v_mfma_f32_16x16x32_bf16 a[192:195], a[88:91], a[132:135], a[192:195]\n"
    "v_rcp_f32 v20, v20\n"
    "v_mfma_f32_16x16x32_bf16 a[192:195], a[8:11], a[112:115], a[192:195]\n"
    "v_rcp_f32 v21, v21\n"
    "v_mfma_f32_16x16x32_bf16 a[192:195], a[24:27], a[116:119], a[192:195]\n"
    "v_rcp_f32 v22, v22\n"
    "v_rcp_f32 v23, v23\n"
    "v_accvgpr_read_b32 v112, a184\n"
    "v_accvgpr_read_b32 v113, a185\n"
    "v_accvgpr_read_b32 v114, a186\n"
    "v_accvgpr_read_b32 v115, a187\n"
    "v_accvgpr_read_b32 v116, a188\n"
    "v_accvgpr_read_b32 v117, a189\n"
    "v_accvgpr_read_b32 v118, a190\n"
    "v_accvgpr_read_b32 v119, a191\n"
    "v_pk_fma_f32 v[112:113], v[112:113], v[32:33], v[48:49]\n"
    "v_pk_fma_f32 v[114:115], v[114:115], v[34:35], v[50:51]\n"
    "v_pk_fma_f32 v[116:117], v[116:117], v[36:37], v[52:53]\n"
    "v_pk_fma_f32 v[118:119], v[118:119], v[38:39], v[54:55]\n"
    "v_pk_mul_f32 v[96:97], v[96:97], v[16:17]\n"
    "v_pk_mul_f32 v[98:99], v[98:99], v[18:19]\n"
    "v_pk_mul_f32 v[100:101], v[100:101], v[20:21]\n"
    "v_pk_mul_f32 v[102:103], v[102:103], v[22:23]\n"
    "v_pk_fma_f32 v[8:9], v[8:9], s[54:55], s[54:55]\n"
    "v_pk_fma_f32 v[10:11], v[10:11], s[54:55], s[54:55]\n"
    "v_pk_fma_f32 v[12:13], v[12:13], s[54:55], s[54:55]\n"
    "v_pk_fma_f32 v[14:15], v[14:15], s[54:55], s[54:55]\n"
    "v_cvt_pk_bf16_f32 v96, v96, v97\n"
    "v_cvt_pk_bf16_f32 v97, v98, v99\n"
    "v_exp_f32 v24, v112\n"
    "v_mfma_f32_16x16x32_bf16 a[196:199], a[44:47], a[120:123], a[196:199]\n"
    "v_cvt_pk_bf16_f32 v98, v100, v101\n"
    "v_cvt_pk_bf16_f32 v99, v102, v103\n"
    "buffer_store_dwordx4 v[96:99], v1, s[64:67], s51 offen offset:0 sc1\n"
    "v_exp_f32 v25, v113\n"
    "v_exp_f32 v26, v114\n"
    "v_mfma_f32_16x16x32_bf16 a[196:199], a[60:63], a[124:127], a[196:199]\n"
    "v_exp_f32 v27, v115\n"
    "v_exp_f32 v28, v116\n"
    "v_exp_f32 v29, v117\n"
    "v_mfma_f32_16x16x32_bf16 a[196:199], a[76:79], a[128:131], a[196:199]\n"
    "v_exp_f32 v30, v118\n"
    "v_exp_f32 v31, v119\n"
    "v_rcp_f32 v8, v8\n"
    "v_mfma_f32_16x16x32_bf16 a[196:199], a[92:95], a[132:135], a[196:199]\n"
    "v_rcp_f32 v9, v9\n"
    "v_rcp_f32 v10, v10\n"
    "v_rcp_f32 v11, v11\n"
    "v_mfma_f32_16x16x32_bf16 a[196:199], a[12:15], a[112:115], a[196:199]\n"
    "v_rcp_f32 v12, v12\n"
    "v_rcp_f32 v13, v13\n"
    "v_mfma_f32_16x16x32_bf16 a[196:199], a[28:31], a[116:119], a[196:199]\n"
    "v_rcp_f32 v14, v14\n"
    "v_rcp_f32 v15, v15\n"
    "v_accvgpr_read_b32 v120, a192\n"
    "v_accvgpr_read_b32 v121, a193\n"
    "v_accvgpr_read_b32 v122, a194\n"
    "v_accvgpr_read_b32 v123, a195\n"
    "s_nop 5\n"
    "v_accvgpr_read_b32 v124, a196\n"
    "v_accvgpr_read_b32 v125, a197\n"
    "v_accvgpr_read_b32 v126, a198\n"
    "v_accvgpr_read_b32 v127, a199\n"
    "v_pk_fma_f32 v[120:121], v[120:121], v[40:41], v[56:57]\n"
    "v_pk_fma_f32 v[122:123], v[122:123], v[42:43], v[58:59]\n"
    "v_pk_fma_f32 v[124:125], v[124:125], v[44:45], v[60:61]\n"
    "v_pk_fma_f32 v[126:127], v[126:127], v[46:47], v[62:63]\n"
    "v_pk_mul_f32 v[104:105], v[104:105], v[8:9]\n"
    "v_pk_mul_f32 v[106:107], v[106:107], v[10:11]\n"
    "v_pk_mul_f32 v[108:109], v[108:109], v[12:13]\n"
    "v_pk_mul_f32 v[110:111], v[110:111], v[14:15]\n"
    "v_pk_fma_f32 v[24:25], v[24:25], s[54:55], s[54:55]\n"
    "v_pk_fma_f32 v[26:27], v[26:27], s[54:55], s[54:55]\n"
    "v_pk_fma_f32 v[28:29], v[28:29], s[54:55], s[54:55]\n"
    "v_pk_fma_f32 v[30:31], v[30:31], s[54:55], s[54:55]\n"
    "v_cvt_pk_bf16_f32 v104, v104, v105\n"
    "v_cvt_pk_bf16_f32 v105, v106, v107\n"
    "v_cvt_pk_bf16_f32 v106, v108, v109\n"
    "v_cvt_pk_bf16_f32 v107, v110, v111\n"
    "buffer_store_dwordx4 v[104:107], v1, s[64:67], s51 offen offset:64 sc1\n"
    "s_add_u32 s51, s51, s52\n"
    "v_exp_f32 v16, v120\n"
    "v_exp_f32 v17, v121\n"
    "v_exp_f32 v18, v122\n"
    "v_exp_f32 v19, v123\n"
    "v_exp_f32 v20, v124\n"
    "v_exp_f32 v21, v125\n"
    "v_exp_f32 v22, v126\n"
    "v_exp_f32 v23, v127\n"
    "v_rcp_f32 v24, v24\n"
    "v_rcp_f32 v25, v25\n"
    "v_rcp_f32 v26, v26\n"
    "v_rcp_f32 v27, v27\n"
    "v_rcp_f32 v28, v28\n"
    "v_rcp_f32 v29, v29\n"
    "v_rcp_f32 v30, v30\n"
    "v_rcp_f32 v31, v31\n"
    "s_nop 0\n"
    "v_pk_mul_f32 v[112:113], v[112:113], v[24:25]\n"
    "v_pk_mul_f32 v[114:115], v[114:115], v[26:27]\n"
    "v_pk_mul_f32 v[116:117], v[116:117], v[28:29]\n"
    "v_pk_mul_f32 v[118:119], v[118:119], v[30:31]\n"
    "v_pk_fma_f32 v[16:17], v[16:17], s[54:55], s[54:55]\n"
    "v_pk_fma_f32 v[18:19], v[18:19], s[54:55], s[54:55]\n"
    "v_pk_fma_f32 v[20:21], v[20:21], s[54:55], s[54:55]\n"
    "v_pk_fma_f32 v[22:23], v[22:23], s[54:55], s[54:55]\n"
    "v_cvt_pk_bf16_f32 v112, v112, v113\n"
    "v_cvt_pk_bf16_f32 v113, v114, v115\n"
    "v_cvt_pk_bf16_f32 v114, v116, v117\n"
    "v_cvt_pk_bf16_f32 v115, v118, v119\n"
    "buffer_store_dwordx4 v[112:115], v1, s[64:67], s51 offen offset:0 sc1\n"
    "v_rcp_f32 v16, v16\n"
    "v_rcp_f32 v17, v17\n"
    "v_rcp_f32 v18, v18\n"
    "v_rcp_f32 v19, v19\n"
    "v_rcp_f32 v20, v20\n"
    "v_rcp_f32 v21, v21\n"
    "v_rcp_f32 v22, v22\n"
    "v_rcp_f32 v23, v23\n"
    "s_nop 0\n"
    "v_pk_mul_f32 v[120:121], v[120:121], v[16:17]\n"
    "v_pk_mul_f32 v[122:123], v[122:123], v[18:19]\n"
    "v_pk_mul_f32 v[124:125], v[124:125], v[20:21]\n"
    "v_pk_mul_f32 v[126:127], v[126:127], v[22:23]\n"
    "v_cvt_pk_bf16_f32 v120, v120, v121\n"
    "v_cvt_pk_bf16_f32 v121, v122, v123\n"
    "v_cvt_pk_bf16_f32 v122, v124, v125\n"
    "v_cvt_pk_bf16_f32 v123, v126, v127\n"
    "buffer_store_dwordx4 v[120:123], v1, s[64:67], s51 offen offset:64 sc1\n"
    "s_add_u32 s51, s51, s52\n"
    "s_nop 0\n"
    "CBS_ENDL:\n"
    :
    : [tid] "v"(threadIdx.x), [bid] "s"(blockIdx.x), [lb] "s"(lb), [ka] "s"(__builtin_amdgcn_kernarg_segment_ptr())
    : "v1", "v2", "v3", "v4", "v5", "v6", "v7", "v8", "v9", "v10", "v11", "v12", "v13", "v14", "v15", "v16", "v17", "v18", "v19", "v20", "v21", "v22", "v23", "v24", "v25", "v26", "v27", "v28", "v29", "v30", "v31", "v32", "v33", "v34", "v35", "v36", "v37", "v38", "v39", "v40", "v41", "v42", "v43", "v44", "v45", "v46", "v47", "v48", "v49", "v50", "v51", "v52", "v53", "v54", "v55", "v56", "v57", "v58", "v59", "v60", "v61", "v62", "v63", "v64", "v65", "v66", "v67", "v68", "v69", "v70", "v71", "v72", "v73", "v74", "v75", "v76", "v77", "v78", "v79", "v80", "v81", "v82", "v83", "v84", "v85", "v86", "v87", "v88", "v89", "v90", "v91", "v92", "v93", "v94", "v95", "v96", "v97", "v98", "v99", "v100", "v101", "v102", "v103", "v104", "v105", "v106", "v107", "v108", "v109", "v110", "v111", "v112", "v113", "v114", "v115", "v116", "v117", "v118", "v119", "v120", "v121", "v122", "v123", "v124", "v125", "v126", "v127", "v128", "v129", "v130", "v131", "v132", "v133", "v134", "v135", "v136", "v137", "v138", "v139", "v140", "v141", "v142", "v143", "v144", "v145", "v146", "v147", "v148", "v149", "v150", "v151", "v152", "v153", "v154", "v155", "v156", "v157", "v158", "v159", "v160", "v161", "v162", "v163", "v164", "v165", "v166", "v167", "v168", "v169", "v170", "v171", "v172", "v173", "v174", "v175", "v176", "v177", "v178", "v179", "v180", "v181", "v182", "v183", "v184", "v185", "v186", "v187", "v188", "v189", "v190", "v191", "v192", "v193", "v194", "v195", "v196", "v197", "v198", "v199", "v200", "v201", "v202", "v203", "v204", "v205", "v206", "v207", "v208", "v209", "v210", "v211", "v212", "v213", "v214", "v215", "v216", "v217", "v218", "v219", "v220", "v221", "v222", "v223", "v224", "v225", "v226", "v227", "v228", "v229", "v230", "v231", "v232", "v233", "v234", "v235", "v236", "v237", "v238", "v239", "v240", "v241", "v242", "v243", "v244", "v245", "v246", "v247", "v248", "v249", "v250", "v251", "v252", "v253", "v254", "v255", "a0", "a1", "a2", "a3", "a4", "a5", "a6", "a7", "a8", "a9", "a10", "a11", "a12", "a13", "a14", "a15", "a16", "a17", "a18", "a19", "a20", "a21", "a22", "a23", "a24", "a25", "a26", "a27", "a28", "a29", "a30", "a31", "a32", "a33", "a34", "a35", "a36", "a37", "a38", "a39", "a40", "a41", "a42", "a43", "a44", "a45", "a46", "a47", "a48", "a49", "a50", "a51", "a52", "a53", "a54", "a55", "a56", "a57", "a58", "a59", "a60", "a61", "a62", "a63", "a64", "a65", "a66", "a67", "a68", "a69", "a70", "a71", "a72", "a73", "a74", "a75", "a76", "a77", "a78", "a79", "a80", "a81", "a82", "a83", "a84", "a85", "a86", "a87", "a88", "a89", "a90", "a91", "a92", "a93", "a94", "a95", "a96", "a97", "a98", "a99", "a100", "a101", "a102", "a103", "a104", "a105", "a106", "a107", "a108", "a109", "a110", "a111", "a112", "a113", "a114", "a115", "a116", "a117", "a118", "a119", "a120", "a121", "a122", "a123", "a124", "a125", "a126", "a127", "a128", "a129", "a130", "a131", "a132", "a133", "a134", "a135", "a136", "a137", "a138", "a139", "a140", "a141", "a142", "a143", "a144", "a145", "a146", "a147", "a148", "a149", "a150", "a151", "a152", "a153", "a154", "a155", "a156", "a157", "a158", "a159", "a160", "a161", "a162", "a163", "a164", "a165", "a166", "a167", "a168", "a169", "a170", "a171", "a172", "a173", "a174", "a175", "a176", "a177", "a178", "a179", "a180", "a181", "a182", "a183", "a184", "a185", "a186", "a187", "a188", "a189", "a190", "a191", "a192", "a193", "a194", "a195", "a196", "a197", "a198", "a199", "s36", "s37", "s38", "s39", "s40", "s41", "s42", "s43", "s44", "s45", "s46", "s47", "s48", "s49", "s50", "s51", "s52", "s53", "s54", "s55", "s56", "s57", "s58", "s59", "s60", "s61", "s62", "s63", "s64", "s65", "s66", "s67", "s68", "s69", "s70", "s71", "s72", "s73", "s74", "s75", "s76", "s77", "s78", "s79", "s80", "s81", "s82", "s83", "s84", "s85", "s86", "s87", "s88", "s89", "s90", "s91", "s92", "s93", "s94", "s95", "s96", "s97", "s98", "s99", "vcc", "scc", "memory");
}

extern "C" void kernel_launch(void* const* d_in, const int* in_sizes, int n_in, void* d_out, int out_size, void* d_ws, size_t ws_size,
                              hipStream_t stream) {
  conv_bn_silu_kernel<<<256, 256, 0, stream>>>((const unsigned short*)d_in[0], (const unsigned short*)d_in[1], (const unsigned short*)d_in[2],
      (const unsigned short*)d_in[3], (const unsigned short*)d_in[4], (const unsigned short*)d_in[5], (unsigned short*)d_out);
}
```
